```python
import torch

def _softmax(x, axis=-1):
    return torch.softmax(x, dim=axis)
def _gelu(x):
    return torch.nn.functional.gelu(x, approximate="tanh")
def _silu(x):
    return torch.nn.functional.silu(x)
def _sigmoid(x):
    return torch.sigmoid(x)
def _layernorm(x, axis=-1, eps=1e-5):
    m = x.mean(dim=axis, keepdim=True)
    v = x.var(dim=axis, keepdim=True, unbiased=False)
    return (x - m) / torch.sqrt(v + eps)
def _scatter_add(tgt, idx, src, axis=0):
    return tgt.clone().index_add_(axis, idx, src)
def _segment_sum(src, seg, n):
    out = torch.zeros((n,) + src.shape[1:], dtype=src.dtype)
    return out.index_add_(0, seg, src)
def _pad(x, pw):
    flat = tuple(v for pair in reversed(pw) for v in pair)
    return torch.nn.functional.pad(x, flat)

kernel_name = 'parallel_sum__ssm_conv_h3__glu_gated_bilinear__prompt'

def setup_inputs():

    _w = (lambda a: (lambda t: t.to(torch.bfloat16) if t.is_floating_point() else t)(torch.as_tensor(a)))
    def _A_setup():
        B, S, D = 1, 2048, 640
        ks = 4
        return {
            "q": _w((lambda *s: torch.randn(*s, dtype=torch.bfloat16) * 0.05)(B, S, D)),
            "k": _w((lambda *s: torch.randn(*s, dtype=torch.bfloat16) * 0.2)(B, S, D)),
            "v": _w((lambda *s: torch.randn(*s, dtype=torch.bfloat16) * 0.2)(B, S, D)),
            "shift_k": _w((lambda *s: torch.randn(*s, dtype=torch.bfloat16) * 0.2)(D, ks) * 0.5),
            "ssm_k": _w((lambda *s: torch.randn(*s, dtype=torch.bfloat16) * 0.4)(D, S) * (1.0 / S ** 0.5)),
            "ssm_D": _w((lambda *s: torch.randn(*s, dtype=torch.bfloat16) * 0.2)(D)),
        }
    _A_in = _A_setup()

    def _B_setup():
        B, S, D, F = 1, 2048, 640, 1728
        return {
            "x": _w((lambda *s: torch.randn(*s, dtype=torch.bfloat16) * 0.05)(B, S, D)),
            "w_gate": _w((lambda *s: torch.randn(*s, dtype=torch.bfloat16) * 0.2)(D, F) * (D ** -0.5)),
            "w_up": _w((lambda *s: torch.randn(*s, dtype=torch.bfloat16) * 0.2)(D, F) * (D ** -0.5)),
            "w_down": _w((lambda *s: torch.randn(*s, dtype=torch.bfloat16) * 0.2)(F, D) * (F ** -0.5)),
        }
    _B_in = _B_setup()

    return {
        "A_q": _A_in["q"],
        "A_k": _A_in["k"],
        "A_v": _A_in["v"],
        "A_shift_k": _A_in["shift_k"],
        "A_ssm_k": _A_in["ssm_k"],
        "A_ssm_D": _A_in["ssm_D"],
        "B_x": _B_in["x"],
        "B_w_gate": _B_in["w_gate"],
        "B_w_up": _B_in["w_up"],
        "B_w_down": _B_in["w_down"],
    }

def reference(A_q, A_k, A_v, A_shift_k, A_ssm_k, A_ssm_D, B_x, B_w_gate, B_w_up, B_w_down):
    def _A_ref(q, k, v, shift_k, ssm_k, ssm_D):
        B, S, D = q.shape
        ks = shift_k.shape[-1]
        k_pad = _pad(k, ((0, 0), (ks - 1, 0), (0, 0)))
        k_shift = torch.zeros_like(k)
        for t in range(ks):
            k_shift = k_shift + k_pad[:, t:t + S, :] * shift_k[:, ks - 1 - t][None, None, :]
        kv = k_shift * v
        L = 2 * S
        kv_pad = _pad(kv, ((0, 0), (0, S), (0, 0)))
        kf_pad = _pad(ssm_k, ((0, 0), (0, S)))
        kv_f = (lambda x, axes=(-2,-1): torch.fft.rfft2(x.float(), dim=axes))(kv_pad[:, None, :, :], axes=(1, 2))
        kf_f = (lambda x, axes=(-2,-1): torch.fft.rfft2(x.float(), dim=axes))(kf_pad[:, None, :], axes=(1, 2))
        kf_f = (lambda x, *d: x.permute(*d))(kf_f, 1, 2, 0)
        y_f = kv_f * kf_f[None, :, :, :]
        y = (lambda x, s, axes=(-2,-1): torch.fft.irfft2(x, s=s, dim=axes).to(torch.bfloat16))(y_f, (1, L), axes=(1, 2))[:, 0, :S, :]
        y = y + kv * ssm_D[None, None, :]
        return q * y

    def _B_ref(x, w_gate, w_up, w_down):
        g = torch.einsum("bsd,df->bsf", x, w_gate)
        u = torch.einsum("bsd,df->bsf", x, w_up)
        h = g * u
        return torch.einsum("bsf,fd->bsd", h, w_down)

    x = A_q
    xn = _layernorm(x, axis=-1)
    return x + _A_ref(xn, A_k, A_v, A_shift_k, A_ssm_k, A_ssm_D) + _B_ref(xn, B_w_gate, B_w_up, B_w_down)
```

```cpp
#include <hip/hip_runtime.h>
#include <hip/hip_bf16.h>
#include <stdint.h>

typedef __bf16 bf16x8 __attribute__((ext_vector_type(8)));
typedef __bf16 bf16x2 __attribute__((ext_vector_type(2)));
typedef short s16x4 __attribute__((ext_vector_type(4)));
typedef float f32x4 __attribute__((ext_vector_type(4)));
typedef float f32x2 __attribute__((ext_vector_type(2)));
typedef unsigned u32x4 __attribute__((ext_vector_type(4)));
typedef unsigned u32x2 __attribute__((ext_vector_type(2)));
typedef unsigned short u16;
#define LAS __attribute__((address_space(3)))
#define GAS __attribute__((address_space(1)))

constexpr int S_ = 2048, D_ = 640, F_ = 1728;
constexpr int NKS2 = 54;

constexpr size_t WS_HT = 0;
constexpr size_t WS_WDT = WS_HT + (size_t)128 * NKS2 * 1024;
constexpr size_t WS_KVP = WS_WDT + (size_t)40 * NKS2 * 1024;
constexpr size_t WS_YT = WS_KVP + (size_t)D_ * 4096;
constexpr size_t WS_STATS = WS_YT + (size_t)S_ * D_ * 2;
constexpr size_t WS_CNT = WS_STATS + (size_t)S_ * 8;

__device__ __forceinline__ float bf16r(float x) { return (float)(__bf16)x; }
__device__ __forceinline__ unsigned pack_bf16(float lo, float hi) {
  f32x2 f = {lo, hi};
  return __builtin_bit_cast(unsigned, __builtin_convertvector(f, bf16x2));
}
__device__ __forceinline__ float bf_lo(unsigned u) { return __uint_as_float(u << 16); }
__device__ __forceinline__ float bf_hi(unsigned u) { return __uint_as_float(u & 0xffff0000u); }
__device__ __forceinline__ float dot2(unsigned a, unsigned b, float c) {
  return __builtin_amdgcn_fdot2_f32_bf16(__builtin_bit_cast(bf16x2, a), __builtin_bit_cast(bf16x2, b), c, false);
}
__device__ __forceinline__ f32x4 mfma16(u32x4 a, u32x4 b, f32x4 c) {
  return __builtin_amdgcn_mfma_f32_16x16x32_bf16(__builtin_bit_cast(bf16x8, a), __builtin_bit_cast(bf16x8, b), c, 0, 0, 0);
}
__device__ __forceinline__ u32x2 tr_read(const char* lds_base, unsigned off) {
  s16x4 v = __builtin_amdgcn_ds_read_tr16_b64_v4i16((LAS s16x4*)(lds_base + off));
  return __builtin_bit_cast(u32x2, v);
}
__device__ __forceinline__ void glds16(const void* g, char* l) {
  __builtin_amdgcn_global_load_lds((const GAS unsigned*)g, (LAS unsigned*)l, 16, 0, 0);
}
__device__ __forceinline__ void glds16_asm(const void* g, unsigned lds_dst) {
  unsigned keep;
  asm volatile("s_mov_b32 %0, m0\n\ts_mov_b32 m0, %2\n\ts_nop 0\n\tglobal_load_lds_dwordx4 %1, off\n\ts_mov_b32 m0, %0"
               : "=&s"(keep) : "v"(g), "s"(lds_dst) : "memory");
}
__device__ __forceinline__ u32x2 tr_read_a(unsigned addr) {
  s16x4 v = __builtin_amdgcn_ds_read_tr16_b64_v4i16((LAS s16x4*)addr);
  return __builtin_bit_cast(u32x2, v);
}
__device__ __forceinline__ unsigned kv_pos(unsigned g, unsigned alpha, unsigned c) {
  unsigned f = ((g & 1u) << 1) | ((alpha & 1u) ^ (g >> 1));
  return 4u * f + c;
}
__device__ __forceinline__ void kstep_tiles(int ks, int& fA, int& fB) {
  if (ks < 45) { int ft = ks / 3, p = ks - 3 * ft; fA = 112 * ft + 32 * p; fB = fA + 16; }
  else if (ks == 45) { fA = 1680; fB = 1696; }
  else { int e = ks - 46; fA = 224 * e + 96; fB = (e < 7) ? (224 * e + 208) : 1712; }
}

constexpr int NSTG = 3;
constexpr int STGW = 64 * 448;
constexpr int STGB = STGW + 128 * 128;
constexpr int K1_SIDE = NSTG * STGB;
constexpr int K1_CS = K1_SIDE + 20480;
constexpr int K1_LDS = K1_CS + 1024;

template <bool V> struct BoolC { static constexpr bool value = V; };

__device__ __forceinline__ void glds16_saddr(unsigned voff, const void* sbase, unsigned lds_dst) {
  unsigned keep;
  asm volatile("s_mov_b32 %0, m0\n\ts_mov_b32 m0, %3\n\ts_nop 0\n\tglobal_load_lds_dwordx4 %1, %2\n\ts_mov_b32 m0, %0"
               : "=&s"(keep) : "v"(voff), "s"(sbase), "s"(lds_dst) : "memory");
}
__device__ __forceinline__ u32x4 lds_rd128(unsigned addr) { return *(const LAS u32x4*)addr; }

__global__ __launch_bounds__(512, 2) void k1_kernel(const u16* __restrict__ x, const u16* __restrict__ kin,
                                                    const u16* __restrict__ vin, const u16* __restrict__ shk,
                                                    const u16* __restrict__ wg, const u16* __restrict__ wu,
                                                    const u16* __restrict__ wd, char* __restrict__ ws) {
  extern __shared__ __attribute__((aligned(16))) char smem[];
  const int b = blockIdx.x;
  const int w = __builtin_amdgcn_readfirstlane(threadIdx.x >> 6);
  const int lane = threadIdx.x & 63;

  if (w >= 4) {
    if (w == 7) {
      if (b == 0 && lane < 8) {
        __hip_atomic_store((GAS unsigned*)(ws + WS_CNT + lane * 256), 0u, __ATOMIC_RELAXED, __HIP_MEMORY_SCOPE_AGENT);
      }
      u32x4 xr[10];
      {
        const u16* xp = x + (size_t)(b * 8 + (lane >> 3)) * D_ + (lane & 7) * 8;
#pragma unroll
        for (int i = 0; i < 10; ++i) xr[i] = *(const u32x4*)(xp + i * 64);
      }
      const bool dowd = b < 216;
      const int ks = b >> 2, nq = b & 3;
      char* lt = smem + K1_SIDE + 10240;
      if (dowd) {
        int fA, fB;
        kstep_tiles(ks, fA, fB);
#pragma unroll
        for (int i = 0; i < 10; ++i) {
          int idx = i * 64 + lane;
          int r = idx / 20, ch = idx - r * 20;
          int f = (r < 16) ? (fA + r) : (fB + r - 16);
          glds16_asm(wd + (size_t)f * D_ + nq * 160 + ch * 8, (unsigned)(size_t)(LAS char*)(lt + i * 1024));
        }
      }
      __builtin_amdgcn_s_barrier();
      asm volatile("s_waitcnt vmcnt(0)" ::: "memory");
      if (dowd) {
        const int g = lane >> 4, q = (lane & 15) >> 2, p = lane & 3;
        const unsigned rb = (unsigned)((4 * g + q) * 320 + p * 8);
#pragma unroll
        for (int n = 0; n < 10; ++n) {
          u32x2 lo = tr_read(lt, rb + n * 32);
          u32x2 hi = tr_read(lt, rb + n * 32 + 16 * 320);
          u32x4 v = {lo.x, lo.y, hi.x, hi.y};
          *(u32x4*)(ws + WS_WDT + ((size_t)((nq * 10 + n) * NKS2 + ks) * 64 + lane) * 16) = v;
        }
      }
      __builtin_amdgcn_s_barrier();
      float sm = 0.f;
#pragma unroll
      for (int i = 0; i < 10; ++i)
#pragma unroll
        for (int e = 0; e < 4; ++e) sm += bf_lo(xr[i][e]) + bf_hi(xr[i][e]);
      sm += __shfl_xor(sm, 1);
      sm += __shfl_xor(sm, 2);
      sm += __shfl_xor(sm, 4);
      const float mean = sm * (1.0f / D_);
      float sq = 0.f;
#pragma unroll
      for (int i = 0; i < 10; ++i)
#pragma unroll
        for (int e = 0; e < 4; ++e) {
          float d0 = bf_lo(xr[i][e]) - mean, d1 = bf_hi(xr[i][e]) - mean;
          sq = fmaf(d0, d0, sq);
          sq = fmaf(d1, d1, sq);
        }
      sq += __shfl_xor(sq, 1);
      sq += __shfl_xor(sq, 2);
      sq += __shfl_xor(sq, 4);
      const float var = sq * (1.0f / D_);
      if ((lane & 7) == 0) {
        f32x2 st = {bf16r(mean), bf16r(sqrtf(bf16r(bf16r(var) + 1e-5f)))};
        *(f32x2*)(ws + WS_STATS + (size_t)(b * 8 + (lane >> 3)) * 8) = st;
      }
      return;
    }
    const int alpha = b >> 4, cg = b & 15;
    const int u = lane >> 5, m = lane & 31;
    const int cc = min((w - 4) * 2 + u, 4);
    const int d0 = cg * 40 + cc * 8;
    const int s0 = alpha * 128 + 4 * m;
    u32x4 kr[7], vr[4], sw[4];
#pragma unroll
    for (int i = 0; i < 7; ++i) {
      int s = s0 - 3 + i;
      u32x4 t = *(const u32x4*)(kin + (size_t)max(s, 0) * D_ + d0);
      u32x4 z = {0u, 0u, 0u, 0u};
      kr[i] = (s >= 0) ? t : z;
    }
#pragma unroll
    for (int i = 0; i < 4; ++i) vr[i] = *(const u32x4*)(vin + (size_t)(s0 + i) * D_ + d0);
#pragma unroll
    for (int i = 0; i < 4; ++i) sw[i] = *(const u32x4*)(shk + (size_t)d0 * 4 + i * 8);
    char* kb = smem + K1_SIDE + cc * 2048;
    const unsigned wb = (unsigned)((m & 7) * 2);
#pragma unroll
    for (int e = 0; e < 8; ++e) {
      if ((e & 3) == 0) __builtin_amdgcn_s_barrier();
      unsigned t01 = sw[e >> 1][(e & 1) * 2], t23 = sw[e >> 1][(e & 1) * 2 + 1];
      float w0 = bf_lo(t01), w1 = bf_hi(t01), w2 = bf_lo(t23), w3 = bf_hi(t23);
      float kf[7];
#pragma unroll
      for (int i = 0; i < 7; ++i) { unsigned t = kr[i][e >> 1]; kf[i] = (e & 1) ? bf_hi(t) : bf_lo(t); }
#pragma unroll
      for (int c = 0; c < 4; ++c) {
        unsigned t = vr[c][e >> 1];
        float vv = (e & 1) ? bf_hi(t) : bf_lo(t);
        float a = kf[c] * w3;
        a = fmaf(kf[c + 1], w2, a);
        a = fmaf(kf[c + 2], w1, a);
        a = bf16r(fmaf(kf[c + 3], w0, a));
        __bf16 r = (__bf16)(a * vv);
        *(LAS u16*)(kb + e * 256 + kv_pos(m >> 3, alpha, c) * 16 + wb) = __builtin_bit_cast(u16, r);
      }
    }
    asm volatile("s_waitcnt lgkmcnt(0)" ::: "memory");
    {
      const int nch = (w == 6) ? 128 : 256;
#pragma unroll
      for (int i = 0; i < 4; ++i) {
        int idx = i * 64 + lane;
        if (idx < nch) {
          int chl = (w - 4) * 16 + (idx >> 4);
          u32x4 v = *(LAS u32x4*)(smem + K1_SIDE + chl * 256 + (idx & 15) * 16);
          *(u32x4*)(ws + WS_KVP + ((size_t)(cg * 40 + chl) * 16 + alpha) * 256 + (idx & 15) * 16) = v;
        }
      }
    }
    return;
  }

  const int xcd = b & 7, bi = b >> 3;
  const int mt = (xcd >> 2) * 8 + (bi >> 2);
  const int ft = (xcd & 3) * 4 + (bi & 3);
  const int f0 = ft * 112;
  const int g = lane >> 4, q = (lane & 15) >> 2, p = lane & 3;
  const unsigned sbase = (unsigned)(size_t)(LAS char*)smem;

  const u16* bsrc[7];
#pragma unroll
  for (int i = 0; i < 7; ++i) {
    int idx = (w * 7 + i) * 64 + lane;
    int k = idx / 28, pos = idx - k * 28;
    int ch = pos ^ (2 * ((k >> 3) & 1));
    int mat = ch >= 14;
    int col = f0 + (ch - 14 * mat) * 8;
    if (col > F_ - 8) col = F_ - 8;
    bsrc[i] = (mat ? wu : wg) + (size_t)k * F_ + col;
  }
  unsigned avo[4];
#pragma unroll
  for (int i = 0; i < 4; ++i) {
    int row = 8 * i + (lane >> 3);
    avo[i] = (unsigned)(((mt * 128 + w * 32 + row) * D_ + (((lane & 7) ^ ((row >> 1) & 7)) * 8)) * 2);
  }
  const char* xs = (const char*)x;

  unsigned roff[14];
#pragma unroll
  for (int j = 0; j < 7; ++j) {
    int jj = j + 2 * w;
    if (jj >= 7) jj -= 7;
    int base = (8 * g + q) * 448 + (p & 1) * 8 + (p >> 1) * 16;
    roff[j] = sbase + (unsigned)(base + ((jj ^ (g & 1)) * 32));
    roff[7 + j] = sbase + (unsigned)(base + (((jj + 7) ^ (g & 1)) * 32));
  }
  unsigned aoff[2];
#pragma unroll
  for (int kk = 0; kk < 2; ++kk)
    aoff[kk] = sbase + (unsigned)(STGW + (w * 32 + (lane & 15)) * 128 + (((kk * 4 + g) ^ ((lane >> 1) & 7)) * 16));

  f32x4 acc[2][14], accs[2], accc[4];
#pragma unroll
  for (int r = 0; r < 2; ++r) {
#pragma unroll
    for (int c = 0; c < 14; ++c) acc[r][c] = (f32x4){0.f, 0.f, 0.f, 0.f};
    accs[r] = (f32x4){0.f, 0.f, 0.f, 0.f};
  }
#pragma unroll
  for (int i = 0; i < 4; ++i) accc[i] = (f32x4){0.f, 0.f, 0.f, 0.f};
  u32x4 ones = {0x3f803f80u, 0x3f803f80u, 0x3f803f80u, 0x3f803f80u};
  asm volatile("" : "+v"(ones));
  u32x4 cw[2] = {{0u, 0u, 0u, 0u}, {0u, 0u, 0u, 0u}}, cx[2] = {{0u, 0u, 0u, 0u}, {0u, 0u, 0u, 0u}};

  auto piece = [&](int i, unsigned lw) __attribute__((always_inline)) {
    if (i < 7) {
      glds16_asm(bsrc[i], lw + (unsigned)((w * 7 + i) * 1024));
      bsrc[i] += 64 * F_;
    } else {
      glds16_saddr(avo[i - 7], xs, lw + (unsigned)(STGW + (w * 4 + i - 7) * 1024));
    }
  };
#pragma unroll
  for (int i = 0; i < 11; ++i) piece(i, sbase);
  xs += 128;
#pragma unroll
  for (int i = 0; i < 11; ++i) piece(i, sbase + STGB);
  xs += 128;

  int slot = 0;
  auto stage = [&](auto loadc) __attribute__((always_inline)) {
    constexpr bool LOAD = decltype(loadc)::value;
    const unsigned lw = sbase + (unsigned)((slot == 0 ? 2 : slot - 1) * STGB);
    u32x4 xa[4], wf[28];
    auto rd = [&](int t) __attribute__((always_inline)) {
      const int kk = t / 14, c = t % 14;
      u32x2 lo = tr_read_a(roff[c] + kk * 32 * 448);
      u32x2 hi = tr_read_a(roff[c] + kk * 32 * 448 + 4 * 448);
      wf[t] = (u32x4){lo.x, lo.y, hi.x, hi.y};
    };
    xa[0] = lds_rd128(aoff[0]);
    xa[1] = lds_rd128(aoff[0] + 2048);
    rd(0);
    rd(1);
    rd(2);
    acc[0][12] = mfma16(cw[0], cx[0], acc[0][12]);
    acc[1][12] = mfma16(cw[0], cx[1], acc[1][12]);
    acc[0][13] = mfma16(cw[1], cx[0], acc[0][13]);
    acc[1][13] = mfma16(cw[1], cx[1], acc[1][13]);
    xa[2] = lds_rd128(aoff[1]);
    xa[3] = lds_rd128(aoff[1] + 2048);
#pragma unroll
    for (int t = 0; t < 26; ++t) {
      const int kk = t / 14, c = t % 14;
      if (t + 3 < 28) rd(t + 3);
      if (LOAD && (t & 1) == 0 && (t >> 1) < 11) piece(t >> 1, lw);
      acc[0][c] = mfma16(wf[t], xa[kk * 2], acc[0][c]);
      acc[1][c] = mfma16(wf[t], xa[kk * 2 + 1], acc[1][c]);
      if (c == 0 || c == 1 || c == 7 || c == 8) {
        const int ci = (c >= 7) ? (c - 5) : c;
        accc[ci] = mfma16(wf[t], ones, accc[ci]);
      }
      if (c == 3) accs[0] = mfma16(ones, xa[kk * 2], accs[0]);
      if (c == 4) accs[1] = mfma16(ones, xa[kk * 2 + 1], accs[1]);
    }
    cw[0] = wf[26];
    cw[1] = wf[27];
    cx[0] = xa[2];
    cx[1] = xa[3];
    if (LOAD) xs += 128;
    const unsigned dl = (slot == 2) ? (unsigned)(-2 * STGB) : (unsigned)STGB;
    slot = (slot == 2) ? 0 : slot + 1;
#pragma unroll
    for (int c = 0; c < 14; ++c) roff[c] += dl;
    aoff[0] += dl;
    aoff[1] += dl;
  };

#pragma unroll 1
  for (int s = 0; s < 8; ++s) {
    asm volatile("s_waitcnt vmcnt(11) lgkmcnt(0)" ::: "memory");
    __builtin_amdgcn_s_barrier();
    asm volatile("" ::: "memory");
    stage(BoolC<true>{});
  }
#pragma unroll 1
  for (int s = 8; s < 10; ++s) {
    if (s == 8) asm volatile("s_waitcnt vmcnt(11) lgkmcnt(0)" ::: "memory");
    else asm volatile("s_waitcnt vmcnt(0) lgkmcnt(0)" ::: "memory");
    __builtin_amdgcn_s_barrier();
    asm volatile("" ::: "memory");
    stage(BoolC<false>{});
  }
  acc[0][12] = mfma16(cw[0], cx[0], acc[0][12]);
  acc[1][12] = mfma16(cw[0], cx[1], acc[1][12]);
  acc[0][13] = mfma16(cw[1], cx[0], acc[0][13]);
  acc[1][13] = mfma16(cw[1], cx[1], acc[1][13]);

  float* csb = (float*)(smem + K1_CS);
  if ((lane & 15) == 0) {
    int j0 = 2 * w, j1 = 2 * w + 1;
    *(f32x4*)(csb + j0 * 16 + 4 * g) = accc[0];
    *(f32x4*)(csb + 112 + j0 * 16 + 4 * g) = accc[2];
    if (w < 3) {
      *(f32x4*)(csb + j1 * 16 + 4 * g) = accc[1];
      *(f32x4*)(csb + 112 + j1 * 16 + 4 * g) = accc[3];
    }
  }
  float mb[2];
#pragma unroll
  for (int r = 0; r < 2; ++r) mb[r] = bf16r(accs[r][0] * (1.0f / D_));
  asm volatile("s_waitcnt lgkmcnt(0)" ::: "memory");
  __builtin_amdgcn_s_barrier();
  asm volatile("" ::: "memory");
#pragma unroll
  for (int j = 0; j < 7; ++j) {
    int jj = j + 2 * w;
    if (jj >= 7) jj -= 7;
    f32x4 cg4 = *(const f32x4*)(csb + jj * 16 + 4 * g);
    f32x4 cu4 = *(const f32x4*)(csb + 112 + jj * 16 + 4 * g);
    int ks, half;
    bool valid;
    if (ft < 15) {
      valid = true;
      if (jj < 6) { ks = 3 * ft + (jj >> 1); half = jj & 1; }
      else { ks = 46 + (ft >> 1); half = ft & 1; }
    } else {
      valid = jj < 3;
      if (jj < 2) { ks = 45; half = jj; }
      else { ks = 53; half = 1; }
    }
#pragma unroll
    for (int r = 0; r < 2; ++r) {
      float hv[4];
#pragma unroll
      for (int e = 0; e < 4; ++e) {
        float gv = bf16r(acc[r][j][e] - mb[r] * cg4[e]);
        float uv = bf16r(acc[r][7 + j][e] - mb[r] * cu4[e]);
        hv[e] = gv * uv;
      }
      u32x2 o = {pack_bf16(hv[0], hv[1]), pack_bf16(hv[2], hv[3])};
      if (valid) {
        int m16 = mt * 8 + w * 2 + r;
        *(u32x2*)(ws + WS_HT + ((size_t)(m16 * NKS2 + ks) * 64 + lane) * 16 + half * 8) = o;
      }
    }
  }
}

constexpr int K2_LDS = 4 * 20 * 1024;
constexpr int NCONV = 160;

template <int N>
__device__ __forceinline__ float dpp_shr(float v) {
  return __builtin_bit_cast(float, __builtin_amdgcn_update_dpp(0, __builtin_bit_cast(int, v), 0x110 + N, 0xf, 0xf, false));
}
template <int N>
__device__ __forceinline__ float dpp_shl(float v) {
  return __builtin_bit_cast(float, __builtin_amdgcn_update_dpp(0, __builtin_bit_cast(int, v), 0x100 + N, 0xf, 0xf, false));
}

__global__ __launch_bounds__(256, 2) void k2_kernel(const u16* __restrict__ x, const u16* __restrict__ ssmk,
                                                    const u16* __restrict__ ssmd, char* ws, u16* __restrict__ out) {
  extern __shared__ __attribute__((aligned(16))) char smem[];
  const int w = __builtin_amdgcn_readfirstlane(threadIdx.x >> 6);
  const int lane = threadIdx.x & 63;
  const int g = lane >> 4;

  if (blockIdx.x < NCONV) {
    const int cb = blockIdx.x;
    const int d = cb * 4 + w;
    char* lb = smem + w * 9216;
    char* Kd = lb + 256;
    char* Xd = lb + 5120;
#pragma unroll
    for (int i = 0; i < 4; ++i) {
      glds16(ssmk + (size_t)d * S_ + i * 512 + lane * 8, Kd + i * 1024);
      glds16(ws + WS_KVP + (size_t)d * 4096 + i * 1024 + lane * 16, Xd + i * 1024);
    }
    {
      u32x4 z = {0u, 0u, 0u, 0u};
      *(LAS u32x4*)(lb + ((lane < 16) ? lane * 16 : 4352 - 256 + lane * 16)) = z;
    }
    const float dd = bf_lo((unsigned)ssmd[d]);
    asm volatile("s_waitcnt vmcnt(0) lgkmcnt(0)" ::: "memory");

    const int q = (lane & 15) >> 2, p = lane & 3;
    const unsigned kof = (unsigned)(256 - 64 * g - 8 * q + 8 * p);
    const int al = (lane >> 2) & 3, c = lane & 3;
    const unsigned xo0 = (unsigned)(5120 + 256 * al + 16 * kv_pos(g, al, c));
    const unsigned xo1 = (unsigned)(5120 + 256 * al + 16 * kv_pos(g, al + 1, c));

    f32x4 acc[8][4];
#pragma unroll
    for (int t = 0; t < 8; ++t)
#pragma unroll
      for (int G = 0; G < 4; ++G) acc[t][G] = (f32x4){0.f, 0.f, 0.f, 0.f};

#pragma unroll
    for (int ph = 0; ph < 4; ++ph) {
#pragma unroll 1
      for (int mh = 0; mh < 2; ++mh) {
#pragma unroll
        for (int mo = 0; mo < 2; ++mo) {
          const int mu = ph * 4 + mh * 2 + mo;
          u32x4 xf[4];
#pragma unroll
          for (int G = ph; G < 4; ++G)
            xf[G] = *(LAS u32x4*)(lb + (mo ? xo1 : xo0) + 256 * (4 * G - mu));
#pragma unroll
          for (int t = 0; t < 8; ++t) {
            u32x2 lo = tr_read(lb, kof + 256 * mu + 32 * t);
            u32x2 hi = tr_read(lb, kof + 256 * mu + 32 * t - 32);
            u32x4 kf = {lo.x, lo.y, hi.x, hi.y};
#pragma unroll
            for (int G = ph; G < 4; ++G) acc[t][G] = mfma16(xf[G], kf, acc[t][G]);
          }
        }
      }
    }
    const int i16 = lane & 15;
    u16 yo[8][4];
#pragma unroll
    for (int G = 0; G < 4; ++G) {
      float pv[4];
#pragma unroll
      for (int r = 1; r < 4; ++r) {
        float prov = (g == 3) ? ((G > 0) ? acc[7][G > 0 ? G - 1 : 0][r] : 0.f) : acc[7][G][r];
        pv[r] = __shfl(prov, (lane + 48) & 63);
      }
#pragma unroll
      for (int t = 0; t < 8; ++t) {
        float y = acc[t][G][0] + dpp_shr<1>(acc[t][G][1]) + dpp_shr<2>(acc[t][G][2]) + dpp_shr<3>(acc[t][G][3]);
        float p1 = (t > 0) ? acc[t > 0 ? t - 1 : 0][G][1] : pv[1];
        float p2 = (t > 0) ? acc[t > 0 ? t - 1 : 0][G][2] : pv[2];
        float p3 = (t > 0) ? acc[t > 0 ? t - 1 : 0][G][3] : pv[3];
        y += dpp_shl<15>(p1) + dpp_shl<14>(p2) + dpp_shl<13>(p3);
        const unsigned ao = (unsigned)(4 * G + g);
        unsigned kvo = 5120 + 256 * ao + 16 * kv_pos((unsigned)(t >> 1), ao, (unsigned)(i16 & 3)) + 2 * (4 * (t & 1) + (i16 >> 2));
        float kvv = bf_lo((unsigned)*(LAS u16*)(lb + kvo));
        __bf16 r = (__bf16)(bf16r(y) + bf16r(kvv * dd));
        yo[t][G] = __builtin_bit_cast(u16, r);
      }
    }
    __syncthreads();
#pragma unroll
    for (int G = 0; G < 4; ++G)
#pragma unroll
      for (int t = 0; t < 8; ++t) {
        int s = 128 * (4 * G + g) + 16 * t + i16;
        *(LAS u16*)(smem + s * 8 + w * 2) = yo[t][G];
      }
    __syncthreads();
#pragma unroll
    for (int i = 0; i < 8; ++i) {
      int s = threadIdx.x + 256 * i;
      unsigned long long v = *(LAS unsigned long long*)(smem + s * 8);
      size_t o = WS_YT + ((size_t)((s >> 4) * 40 + (cb >> 2)) * 64 + (s & 15) + 16 * (cb & 3)) * 8;
      __hip_atomic_store((GAS unsigned long long*)(ws + o), v, __ATOMIC_RELAXED, __HIP_MEMORY_SCOPE_AGENT);
    }
    asm volatile("s_waitcnt vmcnt(0)" ::: "memory");
    __syncthreads();
    if (threadIdx.x == 0)
      __hip_atomic_fetch_add((GAS unsigned*)(ws + WS_CNT + (cb / 20) * 256), 1u, __ATOMIC_RELAXED, __HIP_MEMORY_SCOPE_AGENT);
    return;
  }

  const int b = blockIdx.x - NCONV;
  const int xcd = b & 7, bi = b >> 3;
  const int mt = (xcd >> 1) * 8 + (bi >> 2);
  const int nt = (xcd & 1) * 4 + (bi & 3);
  const int ks0 = (w < 2) ? w * 14 : 28 + (w - 2) * 13;
  const int nks = (w < 2) ? 14 : 13;
  const char* hp = ws + WS_HT + ((size_t)(mt * 4 * NKS2 + ks0) * 64 + lane) * 16;
  const char* wp = ws + WS_WDT + ((size_t)(nt * 5 * NKS2 + ks0) * 64 + lane) * 16;

  f32x4 acc[4][5];
#pragma unroll
  for (int m = 0; m < 4; ++m)
#pragma unroll
    for (int n = 0; n < 5; ++n) acc[m][n] = (f32x4){0.f, 0.f, 0.f, 0.f};

  constexpr int RING = 4;
  u32x4 hf[RING][4], wf[RING][5];
#define LOADK(kk)                                                                          \
  do {                                                                                     \
    _Pragma("unroll") for (int m = 0; m < 4; ++m)                                          \
        hf[(kk) % RING][m] = *(const u32x4*)(hp + (size_t)(m * NKS2 + (kk)) * 1024);       \
    _Pragma("unroll") for (int n = 0; n < 5; ++n)                                          \
        wf[(kk) % RING][n] = *(const u32x4*)(wp + (size_t)(n * NKS2 + (kk)) * 1024);       \
  } while (0)
  LOADK(0);
  LOADK(1);
  LOADK(2);
#pragma unroll
  for (int kk = 0; kk < 14; ++kk) {
    if (kk + 3 < 13) LOADK(kk + 3);
    else if (kk + 3 == 13) { if (nks == 14) LOADK(kk + 3); }
    if (kk < 13 || nks == 14) {
#pragma unroll
      for (int m = 0; m < 4; ++m)
#pragma unroll
        for (int n = 0; n < 5; ++n) acc[m][n] = mfma16(wf[kk % RING][n], hf[kk % RING][m], acc[m][n]);
    }
  }
#pragma unroll
  for (int m = 0; m < 4; ++m)
#pragma unroll
    for (int n = 0; n < 5; ++n) *(LAS f32x4*)(smem + ((w * 20 + m * 5 + n) * 64 + lane) * 16) = acc[m][n];
  if (threadIdx.x == 0) {
    GAS unsigned* cnt = (GAS unsigned*)(ws + WS_CNT + nt * 256);
    while (__hip_atomic_load(cnt, __ATOMIC_RELAXED, __HIP_MEMORY_SCOPE_AGENT) < 20u) __builtin_amdgcn_s_sleep(2);
  }
  __syncthreads();
  const int m16 = mt * 4 + w;
  const int row = m16 * 16 + (lane & 15);
  const f32x2 st = *(const f32x2*)(ws + WS_STATS + (size_t)row * 8);
  const float rs2 = 1.0f / (st.y * st.y);
#pragma unroll
  for (int n = 0; n < 5; ++n) {
    f32x4 s = *(LAS f32x4*)(smem + ((0 * 20 + w * 5 + n) * 64 + lane) * 16);
#pragma unroll
    for (int ww = 1; ww < 4; ++ww) s += *(LAS f32x4*)(smem + ((ww * 20 + w * 5 + n) * 64 + lane) * 16);
    const int n16 = nt * 5 + n;
    const int col = n16 * 16 + 4 * g;
    unsigned long long yv = __hip_atomic_load((GAS unsigned long long*)(ws + WS_YT + ((size_t)(m16 * 40 + n16) * 64 + lane) * 8),
                                              __ATOMIC_RELAXED, __HIP_MEMORY_SCOPE_AGENT);
    u32x2 xv = *(const u32x2*)(x + (size_t)row * D_ + col);
    unsigned y0 = (unsigned)yv, y1 = (unsigned)(yv >> 32);
    float xs[4] = {bf_lo(xv.x), bf_hi(xv.x), bf_lo(xv.y), bf_hi(xv.y)};
    float ys[4] = {bf_lo(y0), bf_hi(y0), bf_lo(y1), bf_hi(y1)};
    float o[4];
#pragma unroll
    for (int e = 0; e < 4; ++e) {
      float xn = bf16r(bf16r(xs[e] - st.x) / st.y);
      float t = bf16r(xs[e] + bf16r(xn * ys[e]));
      o[e] = t + bf16r(s[e] * rs2);
    }
    u32x2 ov = {pack_bf16(o[0], o[1]), pack_bf16(o[2], o[3])};
    *(u32x2*)(out + (size_t)row * D_ + col) = ov;
  }
}

extern "C" void kernel_launch(void* const* d_in, const int* in_sizes, int n_in, void* d_out, int out_size, void* d_ws,
                              size_t ws_size, hipStream_t stream) {
  const u16* q = (const u16*)d_in[0];
  const u16* k = (const u16*)d_in[1];
  const u16* v = (const u16*)d_in[2];
  const u16* shk = (const u16*)d_in[3];
  const u16* ssmk = (const u16*)d_in[4];
  const u16* ssmd = (const u16*)d_in[5];
  const u16* wg = (const u16*)d_in[7];
  const u16* wu = (const u16*)d_in[8];
  const u16* wd = (const u16*)d_in[9];
  (void)hipFuncSetAttribute((const void*)k1_kernel, hipFuncAttributeMaxDynamicSharedMemorySize, K1_LDS);
  (void)hipFuncSetAttribute((const void*)k2_kernel, hipFuncAttributeMaxDynamicSharedMemorySize, K2_LDS);
  k1_kernel<<<256, 512, K1_LDS, stream>>>(q, k, v, shk, wg, wu, wd, (char*)d_ws);
  k2_kernel<<<NCONV + 256, 256, K2_LDS, stream>>>(q, ssmk, ssmd, (char*)d_ws, (u16*)d_out);
}
```

```cpp
#include <hip/hip_runtime.h>
#include <hip/hip_bf16.h>
#include <stdint.h>

typedef __bf16 bf16x8 __attribute__((ext_vector_type(8)));
typedef __bf16 bf16x2 __attribute__((ext_vector_type(2)));
typedef short s16x4 __attribute__((ext_vector_type(4)));
typedef float f32x4 __attribute__((ext_vector_type(4)));
typedef float f32x2 __attribute__((ext_vector_type(2)));
typedef unsigned u32x4 __attribute__((ext_vector_type(4)));
typedef unsigned u32x2 __attribute__((ext_vector_type(2)));
typedef unsigned short u16;
#define LAS __attribute__((address_space(3)))
#define GAS __attribute__((address_space(1)))

constexpr int S_ = 2048, D_ = 640, F_ = 1728;
constexpr int NKS2 = 54;

constexpr size_t WS_HT = 0;
constexpr size_t WS_WDT = WS_HT + (size_t)128 * NKS2 * 1024;
constexpr size_t WS_KVP = WS_WDT + (size_t)40 * NKS2 * 1024;
constexpr size_t WS_YT = WS_KVP + (size_t)D_ * 4096;
constexpr size_t WS_STATS = WS_YT + (size_t)S_ * D_ * 2;
constexpr size_t WS_CNT = WS_STATS + (size_t)S_ * 8;

__device__ __forceinline__ float bf16r(float x) { return (float)(__bf16)x; }
__device__ __forceinline__ unsigned pack_bf16(float lo, float hi) {
  f32x2 f = {lo, hi};
  return __builtin_bit_cast(unsigned, __builtin_convertvector(f, bf16x2));
}
__device__ __forceinline__ float bf_lo(unsigned u) { return __uint_as_float(u << 16); }
__device__ __forceinline__ float bf_hi(unsigned u) { return __uint_as_float(u & 0xffff0000u); }
__device__ __forceinline__ float dot2(unsigned a, unsigned b, float c) {
  return __builtin_amdgcn_fdot2_f32_bf16(__builtin_bit_cast(bf16x2, a), __builtin_bit_cast(bf16x2, b), c, false);
}
__device__ __forceinline__ f32x4 mfma16(u32x4 a, u32x4 b, f32x4 c) {
  return __builtin_amdgcn_mfma_f32_16x16x32_bf16(__builtin_bit_cast(bf16x8, a), __builtin_bit_cast(bf16x8, b), c, 0, 0, 0);
}
__device__ __forceinline__ u32x2 tr_read(const char* lds_base, unsigned off) {
  s16x4 v = __builtin_amdgcn_ds_read_tr16_b64_v4i16((LAS s16x4*)(lds_base + off));
  return __builtin_bit_cast(u32x2, v);
}
__device__ __forceinline__ void glds16(const void* g, char* l) {
  __builtin_amdgcn_global_load_lds((const GAS unsigned*)g, (LAS unsigned*)l, 16, 0, 0);
}
__device__ __forceinline__ void glds16_asm(const void* g, unsigned lds_dst) {
  unsigned keep;
  asm volatile("s_mov_b32 %0, m0\n\ts_mov_b32 m0, %2\n\ts_nop 0\n\tglobal_load_lds_dwordx4 %1, off\n\ts_mov_b32 m0, %0"
               : "=&s"(keep) : "v"(g), "s"(lds_dst) : "memory");
}
__device__ __forceinline__ u32x2 tr_read_a(unsigned addr) {
  s16x4 v = __builtin_amdgcn_ds_read_tr16_b64_v4i16((LAS s16x4*)addr);
  return __builtin_bit_cast(u32x2, v);
}
__device__ __forceinline__ unsigned kv_pos(unsigned g, unsigned alpha, unsigned c) {
  unsigned f = ((g & 1u) << 1) | ((alpha & 1u) ^ (g >> 1));
  return 4u * f + c;
}
__device__ __forceinline__ void kstep_tiles(int ks, int& fA, int& fB) {
  if (ks < 45) { int ft = ks / 3, p = ks - 3 * ft; fA = 112 * ft + 32 * p; fB = fA + 16; }
  else if (ks == 45) { fA = 1680; fB = 1696; }
  else { int e = ks - 46; fA = 224 * e + 96; fB = (e < 7) ? (224 * e + 208) : 1712; }
}

#define ST2_DECL
#define ST2(i)
#define ST2_RT(i)
#define ST2_OUT()
#define STAMP(a, b)
#define STAMP_RT(a, b)
#define STAMP_OUT(a, b)
#define DBG_LDS 0
constexpr int NSTG = 3;
constexpr int STGW = 64 * 448;
constexpr int STGB = STGW + 128 * 128;
constexpr int K1_SIDE = NSTG * STGB;
constexpr int K1_CS = K1_SIDE + 20480;
constexpr int K1_DBG = K1_CS + 1024;
constexpr int K1_LDS = K1_DBG + DBG_LDS;

#define SBAR() __builtin_amdgcn_sched_barrier(0)
template <bool V> struct BoolC { static constexpr bool value = V; };

__device__ __forceinline__ void glds16_saddr(unsigned voff, const void* sbase, unsigned lds_dst) {
  unsigned keep;
  asm volatile("s_mov_b32 %0, m0\n\ts_mov_b32 m0, %3\n\ts_nop 0\n\tglobal_load_lds_dwordx4 %1, %2\n\ts_mov_b32 m0, %0"
               : "=&s"(keep) : "v"(voff), "s"(sbase), "s"(lds_dst) : "memory");
}
__device__ __forceinline__ u32x4 lds_rd128(unsigned addr) { return *(const LAS u32x4*)addr; }

__global__ __launch_bounds__(512, 2) void k1_kernel(const u16* __restrict__ x, const u16* __restrict__ kin,
                                                    const u16* __restrict__ vin, const u16* __restrict__ shk,
                                                    const u16* __restrict__ wg, const u16* __restrict__ wu,
                                                    const u16* __restrict__ wd, char* __restrict__ ws) {
  extern __shared__ __attribute__((aligned(16))) char smem[];
  const int b = blockIdx.x;
  const int w = __builtin_amdgcn_readfirstlane(threadIdx.x >> 6);
  const int lane = threadIdx.x & 63;
  STAMP_RT(K1_DBG, 0);
  STAMP(K1_DBG, 1);

  if (w >= 4) {
    if (w == 7) {
      if (b == 0 && lane < 8) {
        __hip_atomic_store((GAS unsigned*)(ws + WS_CNT + lane * 256), 0u, __ATOMIC_RELAXED, __HIP_MEMORY_SCOPE_AGENT);
      }
      __builtin_amdgcn_s_barrier();
      u32x4 xr[10];
      {
        const u16* xp = x + (size_t)(b * 8 + (lane >> 3)) * D_ + (lane & 7) * 8;
#pragma unroll
        for (int i = 0; i < 10; ++i) xr[i] = *(const u32x4*)(xp + i * 64);
      }
      const bool dowd = b < 216;
      const int ks = b >> 2, nq = b & 3;
      char* lt = smem + K1_SIDE + 10240;
      if (dowd) {
        int fA, fB;
        kstep_tiles(ks, fA, fB);
#pragma unroll
        for (int i = 0; i < 10; ++i) {
          int idx = i * 64 + lane;
          int r = idx / 20, ch = idx - r * 20;
          int f = (r < 16) ? (fA + r) : (fB + r - 16);
          glds16_asm(wd + (size_t)f * D_ + nq * 160 + ch * 8, (unsigned)(size_t)(LAS char*)(lt + i * 1024));
        }
      }
      __builtin_amdgcn_s_barrier();
      __builtin_amdgcn_s_barrier();
      asm volatile("s_waitcnt vmcnt(0)" ::: "memory");
      if (dowd) {
        const int g = lane >> 4, q = (lane & 15) >> 2, p = lane & 3;
        const unsigned rb = (unsigned)((4 * g + q) * 320 + p * 8);
#pragma unroll
        for (int n = 0; n < 10; ++n) {
          u32x2 lo = tr_read(lt, rb + n * 32);
          u32x2 hi = tr_read(lt, rb + n * 32 + 16 * 320);
          u32x4 v = {lo.x, lo.y, hi.x, hi.y};
          *(u32x4*)(ws + WS_WDT + ((size_t)((nq * 10 + n) * NKS2 + ks) * 64 + lane) * 16) = v;
        }
      }
      __builtin_amdgcn_s_barrier();
      float sm = 0.f;
#pragma unroll
      for (int i = 0; i < 10; ++i)
#pragma unroll
        for (int e = 0; e < 4; ++e) sm += bf_lo(xr[i][e]) + bf_hi(xr[i][e]);
      sm += __shfl_xor(sm, 1);
      sm += __shfl_xor(sm, 2);
      sm += __shfl_xor(sm, 4);
      const float mean = sm * (1.0f / D_);
      float sq = 0.f;
#pragma unroll
      for (int i = 0; i < 10; ++i)
#pragma unroll
        for (int e = 0; e < 4; ++e) {
          float d0 = bf_lo(xr[i][e]) - mean, d1 = bf_hi(xr[i][e]) - mean;
          sq = fmaf(d0, d0, sq);
          sq = fmaf(d1, d1, sq);
        }
      sq += __shfl_xor(sq, 1);
      sq += __shfl_xor(sq, 2);
      sq += __shfl_xor(sq, 4);
      const float var = sq * (1.0f / D_);
      if ((lane & 7) == 0) {
        f32x2 st = {bf16r(mean), bf16r(sqrtf(bf16r(bf16r(var) + 1e-5f)))};
        *(f32x2*)(ws + WS_STATS + (size_t)(b * 8 + (lane >> 3)) * 8) = st;
      }
      STAMP(K1_DBG, 2);
      STAMP_RT(K1_DBG, 31);
      STAMP_OUT(K1_DBG, 0);
      return;
    }
    const int alpha = b >> 4, cg = b & 15;
    const int u = lane >> 5, m = lane & 31;
    const int cc = min((w - 4) * 2 + u, 4);
    const int d0 = cg * 40 + cc * 8;
    const int s0 = alpha * 128 + 4 * m;
    u32x4 kr[7], vr[4], sw[4];
    __builtin_amdgcn_s_barrier();
#pragma unroll
    for (int i = 0; i < 7; ++i) {
      int s = s0 - 3 + i;
      u32x4 t = *(const u32x4*)(kin + (size_t)max(s, 0) * D_ + d0);
      u32x4 z = {0u, 0u, 0u, 0u};
      kr[i] = (s >= 0) ? t : z;
    }
#pragma unroll
    for (int i = 0; i < 4; ++i) vr[i] = *(const u32x4*)(vin + (size_t)(s0 + i) * D_ + d0);
#pragma unroll
    for (int i = 0; i < 4; ++i) sw[i] = *(const u32x4*)(shk + (size_t)d0 * 4 + i * 8);
    char* kb = smem + K1_SIDE + cc * 2048;
    const unsigned wb = (unsigned)((m & 7) * 2);
    __builtin_amdgcn_s_barrier();
#pragma unroll
    for (int e = 0; e < 8; ++e) {
      if ((e & 3) == 0) __builtin_amdgcn_s_barrier();
      unsigned t01 = sw[e >> 1][(e & 1) * 2], t23 = sw[e >> 1][(e & 1) * 2 + 1];
      float w0 = bf_lo(t01), w1 = bf_hi(t01), w2 = bf_lo(t23), w3 = bf_hi(t23);
      float kf[7];
#pragma unroll
      for (int i = 0; i < 7; ++i) { unsigned t = kr[i][e >> 1]; kf[i] = (e & 1) ? bf_hi(t) : bf_lo(t); }
#pragma unroll
      for (int c = 0; c < 4; ++c) {
        unsigned t = vr[c][e >> 1];
        float vv = (e & 1) ? bf_hi(t) : bf_lo(t);
        float a = kf[c] * w3;
        a = fmaf(kf[c + 1], w2, a);
        a = fmaf(kf[c + 2], w1, a);
        a = bf16r(fmaf(kf[c + 3], w0, a));
        __bf16 r = (__bf16)(a * vv);
        *(LAS u16*)(kb + e * 256 + kv_pos(m >> 3, alpha, c) * 16 + wb) = __builtin_bit_cast(u16, r);
      }
    }
    asm volatile("s_waitcnt lgkmcnt(0)" ::: "memory");
    {
      const int nch = (w == 6) ? 128 : 256;
#pragma unroll
      for (int i = 0; i < 4; ++i) {
        int idx = i * 64 + lane;
        if (idx < nch) {
          int chl = (w - 4) * 16 + (idx >> 4);
          u32x4 v = *(LAS u32x4*)(smem + K1_SIDE + chl * 256 + (idx & 15) * 16);
          *(u32x4*)(ws + WS_KVP + ((size_t)(cg * 40 + chl) * 16 + alpha) * 256 + (idx & 15) * 16) = v;
        }
      }
    }
    STAMP(K1_DBG, 2);
    STAMP_RT(K1_DBG, 31);
    STAMP_OUT(K1_DBG, 0);
    return;
  }

  const int xcd = b & 7, bi = b >> 3;
  const int mt = (xcd >> 2) * 8 + (bi >> 2);
  const int ft = (xcd & 3) * 4 + (bi & 3);
  const int f0 = ft * 112;
  const int g = lane >> 4, q = (lane & 15) >> 2, p = lane & 3;
  const unsigned sbase = (unsigned)(size_t)(LAS char*)smem;

  const u16* bsrc[7];
#pragma unroll
  for (int i = 0; i < 7; ++i) {
    int idx = (w * 7 + i) * 64 + lane;
    int k = idx / 28, pos = idx - k * 28;
    int ch = pos ^ (2 * ((k >> 3) & 1));
    int mat = ch >= 14;
    int col = f0 + (ch - 14 * mat) * 8;
    if (col > F_ - 8) col = F_ - 8;
    bsrc[i] = (mat ? wu : wg) + (size_t)k * F_ + col;
  }
  unsigned avo[4];
#pragma unroll
  for (int i = 0; i < 4; ++i) {
    int row = 8 * i + (lane >> 3);
    avo[i] = (unsigned)(((mt * 128 + w * 32 + row) * D_ + (((lane & 7) ^ ((row >> 1) & 7)) * 8)) * 2);
  }
  const char* xs = (const char*)x;

  unsigned roff[14];
#pragma unroll
  for (int j = 0; j < 7; ++j) {
    int jj = j;
    if (w < 3) {
      if (j < 2) jj = 2 * w + j;
      else if ((j >> 1) == w) jj = j & 1;
    } else {
      if (j == 0) jj = 6;
      else if (j == 6) jj = 0;
    }
    int base = (8 * g + q) * 448 + (p & 1) * 8 + (p >> 1) * 16;
    roff[j] = sbase + (unsigned)(base + ((jj ^ (g & 1)) * 32));
    roff[7 + j] = sbase + (unsigned)(base + (((jj + 7) ^ (g & 1)) * 32));
  }
  unsigned aoff[2];
#pragma unroll
  for (int kk = 0; kk < 2; ++kk)
    aoff[kk] = sbase + (unsigned)(STGW + (w * 32 + (lane & 15)) * 128 + (((kk * 4 + g) ^ ((lane >> 1) & 7)) * 16));

  f32x4 acc[2][14], accs[2], accc[4];
#pragma unroll
  for (int r = 0; r < 2; ++r) {
#pragma unroll
    for (int c = 0; c < 14; ++c) acc[r][c] = (f32x4){0.f, 0.f, 0.f, 0.f};
    accs[r] = (f32x4){0.f, 0.f, 0.f, 0.f};
  }
#pragma unroll
  for (int i = 0; i < 4; ++i) accc[i] = (f32x4){0.f, 0.f, 0.f, 0.f};
  u32x4 ones = {0x3f803f80u, 0x3f803f80u, 0x3f803f80u, 0x3f803f80u};
  asm volatile("" : "+v"(ones));
  u32x4 cw[2] = {{0u, 0u, 0u, 0u}, {0u, 0u, 0u, 0u}}, cx[2] = {{0u, 0u, 0u, 0u}, {0u, 0u, 0u, 0u}};

  auto piece = [&](int i, unsigned lw) __attribute__((always_inline)) {
    if (i < 7) {
      glds16_asm(bsrc[i], lw + (unsigned)((w * 7 + i) * 1024));
      bsrc[i] += 64 * F_;
    } else {
      glds16_saddr(avo[i - 7], xs, lw + (unsigned)(STGW + (w * 4 + i - 7) * 1024));
    }
  };
  STAMP(K1_DBG, 25);
  piece(0, sbase);
  STAMP(K1_DBG, 26);
#pragma unroll
  for (int i = 1; i < 11; ++i) piece(i, sbase);
  xs += 128;
  STAMP(K1_DBG, 27);
#pragma unroll
  for (int i = 0; i < 11; ++i) piece(i, sbase + STGB);
  xs += 128;

  STAMP(K1_DBG, 2);
  int slot = 0;
  auto stage = [&](auto loadc) __attribute__((always_inline)) {
    constexpr bool LOAD = decltype(loadc)::value;
    const unsigned lw = sbase + (unsigned)((slot == 0 ? 2 : slot - 1) * STGB);
    u32x4 xa[4], wf[28];
    auto rd = [&](int t) __attribute__((always_inline)) {
      const int kk = t / 14, c = t % 14;
      u32x2 lo = tr_read_a(roff[c] + kk * 32 * 448);
      u32x2 hi = tr_read_a(roff[c] + kk * 32 * 448 + 4 * 448);
      wf[t] = (u32x4){lo.x, lo.y, hi.x, hi.y};
    };
    xa[0] = lds_rd128(aoff[0]);
    xa[1] = lds_rd128(aoff[0] + 2048);
    rd(0);
    rd(1);
    rd(2);
    SBAR();
    acc[0][12] = mfma16(cw[0], cx[0], acc[0][12]);
    acc[1][12] = mfma16(cw[0], cx[1], acc[1][12]);
    acc[0][13] = mfma16(cw[1], cx[0], acc[0][13]);
    acc[1][13] = mfma16(cw[1], cx[1], acc[1][13]);
    SBAR();
#pragma unroll
    for (int t = 0; t < 26; ++t) {
      const int kk = t / 14, c = t % 14;
      if (t + 3 < 28) rd(t + 3);
      if (t == 6) xa[2] = lds_rd128(aoff[1]);
      if (t == 7) xa[3] = lds_rd128(aoff[1] + 2048);
      SBAR();
      if (LOAD && (t & 1) == 0 && (t >> 1) < 11) piece(t >> 1, lw);
      acc[0][c] = mfma16(wf[t], xa[kk * 2], acc[0][c]);
      acc[1][c] = mfma16(wf[t], xa[kk * 2 + 1], acc[1][c]);
      if (c == 0 || c == 1 || c == 7 || c == 8) {
        const int ci = (c >= 7) ? (c - 5) : c;
        accc[ci] = mfma16(wf[t], ones, accc[ci]);
      }
      if (c == 3) accs[0] = mfma16(ones, xa[kk * 2], accs[0]);
      if (c == 4) accs[1] = mfma16(ones, xa[kk * 2 + 1], accs[1]);
      SBAR();
    }
    cw[0] = wf[26];
    cw[1] = wf[27];
    cx[0] = xa[2];
    cx[1] = xa[3];
    if (LOAD) xs += 128;
    const unsigned dl = (slot == 2) ? (unsigned)(-2 * STGB) : (unsigned)STGB;
    slot = (slot == 2) ? 0 : slot + 1;
#pragma unroll
    for (int c = 0; c < 14; ++c) roff[c] += dl;
    aoff[0] += dl;
    aoff[1] += dl;
  };

#pragma unroll 1
  for (int s = 0; s < 8; ++s) {
    STAMP(K1_DBG, 3 + 2 * s);
    asm volatile("s_waitcnt vmcnt(11) lgkmcnt(0)" ::: "memory");
    __builtin_amdgcn_s_barrier();
    asm volatile("" ::: "memory");
    STAMP(K1_DBG, 4 + 2 * s);
    stage(BoolC<true>{});
  }
#pragma unroll 1
  for (int s = 8; s < 10; ++s) {
    STAMP(K1_DBG, 3 + 2 * s);
    if (s == 8) asm volatile("s_waitcnt vmcnt(11) lgkmcnt(0)" ::: "memory");
    else asm volatile("s_waitcnt vmcnt(0) lgkmcnt(0)" ::: "memory");
    __builtin_amdgcn_s_barrier();
    asm volatile("" ::: "memory");
    STAMP(K1_DBG, 4 + 2 * s);
    stage(BoolC<false>{});
  }
  acc[0][12] = mfma16(cw[0], cx[0], acc[0][12]);
  acc[1][12] = mfma16(cw[0], cx[1], acc[1][12]);
  acc[0][13] = mfma16(cw[1], cx[0], acc[0][13]);
  acc[1][13] = mfma16(cw[1], cx[1], acc[1][13]);

  STAMP(K1_DBG, 23);
  float* csb = (float*)(smem + K1_CS);
  if ((lane & 15) == 0) {
    *(f32x4*)(csb + 2 * w * 16 + 4 * g) = accc[0];
    *(f32x4*)(csb + 112 + 2 * w * 16 + 4 * g) = accc[2];
    if (w < 3) {
      *(f32x4*)(csb + (2 * w + 1) * 16 + 4 * g) = accc[1];
      *(f32x4*)(csb + 112 + (2 * w + 1) * 16 + 4 * g) = accc[3];
    }
  }
  float mb[2];
#pragma unroll
  for (int r = 0; r < 2; ++r) mb[r] = bf16r(accs[r][0] * (1.0f / D_));
  if (w == 3) {
#pragma unroll
    for (int r = 0; r < 2; ++r) {
      f32x4 t = acc[r][0]; acc[r][0] = acc[r][6]; acc[r][6] = t;
      t = acc[r][7]; acc[r][7] = acc[r][13]; acc[r][13] = t;
    }
  }
  asm volatile("s_waitcnt lgkmcnt(0)" ::: "memory");
  __builtin_amdgcn_s_barrier();
  asm volatile("" ::: "memory");
  char* hb = ws + WS_HT + ((size_t)((mt * 8 + w * 2) * NKS2) * 64 + lane) * 16;
  auto htile = [&](int r, int j, int jj) __attribute__((always_inline)) -> u32x2 {
    f32x4 cg4 = *(const f32x4*)(csb + jj * 16 + 4 * g);
    f32x4 cu4 = *(const f32x4*)(csb + 112 + jj * 16 + 4 * g);
    float hv[4];
#pragma unroll
    for (int e = 0; e < 4; ++e) {
      float gv = bf16r(acc[r][j][e] - mb[r] * cg4[e]);
      float uv = bf16r(acc[r][7 + j][e] - mb[r] * cu4[e]);
      hv[e] = gv * uv;
    }
    return (u32x2){pack_bf16(hv[0], hv[1]), pack_bf16(hv[2], hv[3])};
  };
#pragma unroll
  for (int i = 0; i < 3; ++i) {
    const int tp = (w < 3) ? (i == 0 ? w : (i == w ? 0 : i)) : i;
#pragma unroll
    for (int r = 0; r < 2; ++r) {
      u32x2 a = htile(r, 2 * i, 2 * tp), c = htile(r, 2 * i + 1, 2 * tp + 1);
      if (ft < 15 || tp == 0) {
        *(u32x4*)(hb + (size_t)(r * NKS2 + 3 * ft + tp) * 1024) = (u32x4){a.x, a.y, c.x, c.y};
      } else if (tp == 1) {
        *(u32x2*)(hb + (size_t)(r * NKS2 + 53) * 1024 + 8) = a;
      }
    }
  }
  if (ft < 15) {
#pragma unroll
    for (int r = 0; r < 2; ++r) {
      u32x2 a = htile(r, 6, 6);
      *(u32x2*)(hb + (size_t)(r * NKS2 + 46 + (ft >> 1)) * 1024 + (ft & 1) * 8) = a;
    }
  }
  STAMP(K1_DBG, 24);
  STAMP_RT(K1_DBG, 31);
  STAMP_OUT(K1_DBG, 0);
}

constexpr int K2_LDS = 4 * 20 * 1024;
constexpr int NCONV = 160;

template <int OFF>
__device__ __forceinline__ void gload16s(u32x4& dst, unsigned voff, const void* sbase) {
  asm volatile("global_load_dwordx4 %0, %1, %2 offset:%3" : "=v"(dst) : "v"(voff), "s"(sbase), "i"(OFF) : "memory");
}
template <int OFF>
__device__ __forceinline__ void gload8s(u32x2& dst, unsigned voff, const void* sbase) {
  asm volatile("global_load_dwordx2 %0, %1, %2 offset:%3" : "=v"(dst) : "v"(voff), "s"(sbase), "i"(OFF) : "memory");
}
template <int N>
__device__ __forceinline__ float dpp_shr(float v) {
  return __builtin_bit_cast(float, __builtin_amdgcn_update_dpp(0, __builtin_bit_cast(int, v), 0x110 + N, 0xf, 0xf, false));
}
template <int N>
__device__ __forceinline__ float dpp_shl(float v) {
  return __builtin_bit_cast(float, __builtin_amdgcn_update_dpp(0, __builtin_bit_cast(int, v), 0x100 + N, 0xf, 0xf, false));
}

__global__ __launch_bounds__(256, 2) void k2_kernel(const u16* __restrict__ x, const u16* __restrict__ ssmk,
                                                    const u16* __restrict__ ssmd, char* ws, u16* __restrict__ out) {
  extern __shared__ __attribute__((aligned(16))) char smem[];
  const int w = __builtin_amdgcn_readfirstlane(threadIdx.x >> 6);
  const int lane = threadIdx.x & 63;
  const int g = lane >> 4;
  ST2_DECL;
  ST2_RT(0);
  ST2(1);

  if (blockIdx.x < NCONV) {
    const int cb = blockIdx.x;
    const int d = cb * 4 + w;
    char* lb = smem + w * 9216;
    char* Kd = lb + 256;
    char* Xd = lb + 5120;
#pragma unroll
    for (int i = 0; i < 4; ++i) {
      glds16(ssmk + (size_t)d * S_ + i * 512 + lane * 8, Kd + i * 1024);
      glds16(ws + WS_KVP + (size_t)d * 4096 + i * 1024 + lane * 16, Xd + i * 1024);
    }
    {
      u32x4 z = {0u, 0u, 0u, 0u};
      *(LAS u32x4*)(lb + ((lane < 16) ? lane * 16 : 4352 - 256 + lane * 16)) = z;
    }
    const float dd = bf_lo((unsigned)ssmd[d]);
    asm volatile("s_waitcnt vmcnt(0) lgkmcnt(0)" ::: "memory");
    ST2(2);

    const int q = (lane & 15) >> 2, p = lane & 3;
    const unsigned kof = (unsigned)(256 - 64 * g - 8 * q + 8 * p);
    const int al = (lane >> 2) & 3, c = lane & 3;
    const unsigned xo0 = (unsigned)(5120 + 256 * al + 16 * kv_pos(g, al, c));
    const unsigned xo1 = (unsigned)(5120 + 256 * al + 16 * kv_pos(g, al + 1, c));

    f32x4 acc[8][4];
#pragma unroll
    for (int t = 0; t < 8; ++t)
#pragma unroll
      for (int G = 0; G < 4; ++G) acc[t][G] = (f32x4){0.f, 0.f, 0.f, 0.f};

#pragma unroll
    for (int ph = 0; ph < 4; ++ph) {
#pragma unroll 1
      for (int mh = 0; mh < 2; ++mh) {
#pragma unroll
        for (int mo = 0; mo < 2; ++mo) {
          const int mu = ph * 4 + mh * 2 + mo;
          u32x4 xf[4];
#pragma unroll
          for (int G = ph; G < 4; ++G)
            xf[G] = *(LAS u32x4*)(lb + (mo ? xo1 : xo0) + 256 * (4 * G - mu));
#pragma unroll
          for (int t = 0; t < 8; ++t) {
            u32x2 lo = tr_read(lb, kof + 256 * mu + 32 * t);
            u32x2 hi = tr_read(lb, kof + 256 * mu + 32 * t - 32);
            u32x4 kf = {lo.x, lo.y, hi.x, hi.y};
#pragma unroll
            for (int G = ph; G < 4; ++G) acc[t][G] = mfma16(xf[G], kf, acc[t][G]);
          }
        }
      }
    }
    ST2(3);
    const int i16 = lane & 15;
    u16 yo[8][4];
#pragma unroll
    for (int G = 0; G < 4; ++G) {
      float pv[4];
#pragma unroll
      for (int r = 1; r < 4; ++r) {
        float prov = (g == 3) ? ((G > 0) ? acc[7][G > 0 ? G - 1 : 0][r] : 0.f) : acc[7][G][r];
        pv[r] = __shfl(prov, (lane + 48) & 63);
      }
#pragma unroll
      for (int t = 0; t < 8; ++t) {
        float y = acc[t][G][0] + dpp_shr<1>(acc[t][G][1]) + dpp_shr<2>(acc[t][G][2]) + dpp_shr<3>(acc[t][G][3]);
        float p1 = (t > 0) ? acc[t > 0 ? t - 1 : 0][G][1] : pv[1];
        float p2 = (t > 0) ? acc[t > 0 ? t - 1 : 0][G][2] : pv[2];
        float p3 = (t > 0) ? acc[t > 0 ? t - 1 : 0][G][3] : pv[3];
        y += dpp_shl<15>(p1) + dpp_shl<14>(p2) + dpp_shl<13>(p3);
        const unsigned ao = (unsigned)(4 * G + g);
        unsigned kvo = 5120 + 256 * ao + 16 * kv_pos((unsigned)(t >> 1), ao, (unsigned)(i16 & 3)) + 2 * (4 * (t & 1) + (i16 >> 2));
        float kvv = bf_lo((unsigned)*(LAS u16*)(lb + kvo));
        __bf16 r = (__bf16)(bf16r(y) + bf16r(kvv * dd));
        yo[t][G] = __builtin_bit_cast(u16, r);
      }
    }
    ST2(4);
    __syncthreads();
#pragma unroll
    for (int G = 0; G < 4; ++G)
#pragma unroll
      for (int t = 0; t < 8; ++t) {
        int s = 128 * (4 * G + g) + 16 * t + i16;
        *(LAS u16*)(smem + s * 8 + w * 2) = yo[t][G];
      }
    __syncthreads();
#pragma unroll
    for (int i = 0; i < 8; ++i) {
      int s = threadIdx.x + 256 * i;
      unsigned long long v = *(LAS unsigned long long*)(smem + s * 8);
      size_t o = WS_YT + ((size_t)((s >> 4) * 40 + (cb >> 2)) * 64 + (s & 15) + 16 * (cb & 3)) * 8;
      __hip_atomic_store((GAS unsigned long long*)(ws + o), v, __ATOMIC_RELAXED, __HIP_MEMORY_SCOPE_AGENT);
    }
    ST2(5);
    asm volatile("s_waitcnt vmcnt(0)" ::: "memory");
    __syncthreads();
    if (threadIdx.x == 0)
      __hip_atomic_fetch_add((GAS unsigned*)(ws + WS_CNT + (cb / 20) * 256), 1u, __ATOMIC_RELAXED, __HIP_MEMORY_SCOPE_AGENT);
    ST2(6);
    ST2_RT(11);
    ST2_OUT();
    return;
  }

  const int b = blockIdx.x - NCONV;
  const int xcd = b & 7, bi = b >> 3;
  const int mt = (xcd >> 1) * 8 + (bi >> 2);
  const int nt = (xcd & 1) * 4 + (bi & 3);
  const int ksf = (w < 2) ? w * 14 : 27 + (w - 2) * 13;
  const char* hp = ws + WS_HT + (size_t)(mt * 4 * NKS2 + ksf) * 1024;
  const char* wp = ws + WS_WDT + (size_t)(nt * 5 * NKS2 + ksf) * 1024;
  const int m16 = mt * 4 + w;
  const int row = m16 * 16 + (lane & 15);

  u32x2 xv[5], stv;
  {
    const unsigned xo = (unsigned)((row * D_ + nt * 80 + 4 * g) * 2);
    gload8s<0>(xv[0], xo, x);
    gload8s<32>(xv[1], xo, x);
    gload8s<64>(xv[2], xo, x);
    gload8s<96>(xv[3], xo, x);
    gload8s<128>(xv[4], xo, x);
    gload8s<0>(stv, (unsigned)(row * 8), ws + WS_STATS);
  }

  f32x4 acc[4][5];
#pragma unroll
  for (int m = 0; m < 4; ++m)
#pragma unroll
    for (int n = 0; n < 5; ++n) acc[m][n] = (f32x4){0.f, 0.f, 0.f, 0.f};

  u32x4 hf[4][4], wf[4][5];
  unsigned vo[4];
#pragma unroll
  for (int i = 0; i < 4; ++i) vo[i] = (unsigned)(lane * 16 + i * 4096);
#define LOADK(j)                                                                           \
  do {                                                                                     \
    _Pragma("unroll") for (int m = 0; m < 4; ++m)                                          \
        gload16s<((j) & 3) * 1024>(hf[(j) & 3][m], vo[(j) >> 2], hp + (size_t)m * NKS2 * 1024); \
    _Pragma("unroll") for (int n = 0; n < 5; ++n)                                          \
        gload16s<((j) & 3) * 1024>(wf[(j) & 3][n], vo[(j) >> 2], wp + (size_t)n * NKS2 * 1024); \
  } while (0)
#define WAITK(j, N)                                                                        \
  asm volatile("s_waitcnt vmcnt(" #N ")"                                                   \
               : "+v"(hf[(j) & 3][0]), "+v"(hf[(j) & 3][1]), "+v"(hf[(j) & 3][2]), "+v"(hf[(j) & 3][3]), \
                 "+v"(wf[(j) & 3][0]), "+v"(wf[(j) & 3][1]), "+v"(wf[(j) & 3][2]), "+v"(wf[(j) & 3][3]), \
                 "+v"(wf[(j) & 3][4])::"memory")
#define MMAK(j)                                                                            \
  do {                                                                                     \
    _Pragma("unroll") for (int m = 0; m < 4; ++m)                                          \
        _Pragma("unroll") for (int n = 0; n < 5; ++n)                                      \
            acc[m][n] = mfma16(wf[(j) & 3][n], hf[(j) & 3][m], acc[m][n]);                 \
    SBAR();                                                                                \
  } while (0)
  if (w < 2) LOADK(0);
  LOADK(1);
  LOADK(2);
  LOADK(3);
  if (w < 2) { WAITK(0, 27); MMAK(0); }
  LOADK(4);  WAITK(1, 27);  MMAK(1);
  LOADK(5);  WAITK(2, 27);  MMAK(2);
  LOADK(6);  WAITK(3, 27);  MMAK(3);
  LOADK(7);  WAITK(4, 27);  MMAK(4);
  LOADK(8);  WAITK(5, 27);  MMAK(5);
  LOADK(9);  WAITK(6, 27);  MMAK(6);
  LOADK(10); WAITK(7, 27);  MMAK(7);
  LOADK(11); WAITK(8, 27);  MMAK(8);
  LOADK(12); WAITK(9, 27);  MMAK(9);
  LOADK(13); WAITK(10, 27); MMAK(10);
  WAITK(11, 18); MMAK(11);
  WAITK(12, 9);  MMAK(12);
  WAITK(13, 0);  MMAK(13);
  asm volatile("" : "+v"(xv[0]), "+v"(xv[1]), "+v"(xv[2]), "+v"(xv[3]), "+v"(xv[4]), "+v"(stv));
  ST2(3);
#pragma unroll
  for (int m = 0; m < 4; ++m)
#pragma unroll
    for (int n = 0; n < 5; ++n) *(LAS f32x4*)(smem + ((w * 20 + m * 5 + n) * 64 + lane) * 16) = acc[m][n];
  {
    GAS unsigned* cnt = (GAS unsigned*)(ws + WS_CNT + nt * 256);
    while (__hip_atomic_load(cnt, __ATOMIC_RELAXED, __HIP_MEMORY_SCOPE_AGENT) < 20u) __builtin_amdgcn_s_sleep(2);
  }
  ST2(4);
  unsigned long long yv[5];
#pragma unroll
  for (int n = 0; n < 5; ++n)
    yv[n] = __hip_atomic_load((GAS unsigned long long*)(ws + WS_YT + ((size_t)(m16 * 40 + nt * 5 + n) * 64 + lane) * 8),
                              __ATOMIC_RELAXED, __HIP_MEMORY_SCOPE_AGENT);
  asm volatile("s_waitcnt lgkmcnt(0)" ::: "memory");
  __builtin_amdgcn_s_barrier();
  asm volatile("" ::: "memory");
  ST2(5);
  const float mean = __uint_as_float(stv.x), den = __uint_as_float(stv.y);
  const float rs2 = 1.0f / (den * den);
#pragma unroll
  for (int n = 0; n < 5; ++n) {
    f32x4 s = *(LAS f32x4*)(smem + ((0 * 20 + w * 5 + n) * 64 + lane) * 16);
#pragma unroll
    for (int ww = 1; ww < 4; ++ww) s += *(LAS f32x4*)(smem + ((ww * 20 + w * 5 + n) * 64 + lane) * 16);
    const int col = (nt * 5 + n) * 16 + 4 * g;
    unsigned y0 = (unsigned)yv[n], y1 = (unsigned)(yv[n] >> 32);
    float xs[4] = {bf_lo(xv[n].x), bf_hi(xv[n].x), bf_lo(xv[n].y), bf_hi(xv[n].y)};
    float ys[4] = {bf_lo(y0), bf_hi(y0), bf_lo(y1), bf_hi(y1)};
    float o[4];
#pragma unroll
    for (int e = 0; e < 4; ++e) {
      float xn = bf16r(bf16r(xs[e] - mean) / den);
      float t = bf16r(xs[e] + bf16r(xn * ys[e]));
      o[e] = t + bf16r(s[e] * rs2);
    }
    u32x2 ov = {pack_bf16(o[0], o[1]), pack_bf16(o[2], o[3])};
    *(u32x2*)(out + (size_t)row * D_ + col) = ov;
  }
  ST2(6);
  ST2_RT(11);
  ST2_OUT();
}

extern "C" void kernel_launch(void* const* d_in, const int* in_sizes, int n_in, void* d_out, int out_size, void* d_ws,
                              size_t ws_size, hipStream_t stream) {
  const u16* q = (const u16*)d_in[0];
  const u16* k = (const u16*)d_in[1];
  const u16* v = (const u16*)d_in[2];
  const u16* shk = (const u16*)d_in[3];
  const u16* ssmk = (const u16*)d_in[4];
  const u16* ssmd = (const u16*)d_in[5];
  const u16* wg = (const u16*)d_in[7];
  const u16* wu = (const u16*)d_in[8];
  const u16* wd = (const u16*)d_in[9];
  (void)hipFuncSetAttribute((const void*)k1_kernel, hipFuncAttributeMaxDynamicSharedMemorySize, K1_LDS);
  (void)hipFuncSetAttribute((const void*)k2_kernel, hipFuncAttributeMaxDynamicSharedMemorySize, K2_LDS);
  k1_kernel<<<256, 512, K1_LDS, stream>>>(q, k, v, shk, wg, wu, wd, (char*)d_ws);
  k2_kernel<<<NCONV + 256, 256, K2_LDS, stream>>>(q, ssmk, ssmd, (char*)d_ws, (u16*)d_out);
}
```

```cpp
#include <hip/hip_runtime.h>
#include <hip/hip_bf16.h>
#include <stdint.h>

typedef __bf16 bf16x8 __attribute__((ext_vector_type(8)));
typedef __bf16 bf16x2 __attribute__((ext_vector_type(2)));
typedef short s16x4 __attribute__((ext_vector_type(4)));
typedef float f32x4 __attribute__((ext_vector_type(4)));
typedef float f32x16 __attribute__((ext_vector_type(16)));
typedef float f32x2 __attribute__((ext_vector_type(2)));
typedef unsigned u32x4 __attribute__((ext_vector_type(4)));
typedef unsigned u32x2 __attribute__((ext_vector_type(2)));
typedef unsigned short u16;
#define LAS __attribute__((address_space(3)))
#define GAS __attribute__((address_space(1)))

constexpr int S_ = 2048, D_ = 640, F_ = 1728;
constexpr int NKS2 = 54;

constexpr size_t WS_HT = 0;
constexpr size_t WS_WDT = WS_HT + (size_t)128 * NKS2 * 1024;
constexpr size_t WS_KVP = WS_WDT + (size_t)40 * NKS2 * 1024;
constexpr size_t WS_YT = WS_KVP + (size_t)D_ * 4096;
constexpr size_t WS_STATS = WS_YT + (size_t)S_ * D_ * 2;
constexpr size_t WS_CNT = WS_STATS + (size_t)S_ * 8;

__device__ __forceinline__ float bf16r(float x) { return (float)(__bf16)x; }
__device__ __forceinline__ unsigned pack_bf16(float lo, float hi) {
  f32x2 f = {lo, hi};
  return __builtin_bit_cast(unsigned, __builtin_convertvector(f, bf16x2));
}
__device__ __forceinline__ float bf_lo(unsigned u) { return __uint_as_float(u << 16); }
__device__ __forceinline__ float bf_hi(unsigned u) { return __uint_as_float(u & 0xffff0000u); }
__device__ __forceinline__ float dot2(unsigned a, unsigned b, float c) {
  return __builtin_amdgcn_fdot2_f32_bf16(__builtin_bit_cast(bf16x2, a), __builtin_bit_cast(bf16x2, b), c, false);
}
__device__ __forceinline__ f32x4 mfma16(u32x4 a, u32x4 b, f32x4 c) {
  return __builtin_amdgcn_mfma_f32_16x16x32_bf16(__builtin_bit_cast(bf16x8, a), __builtin_bit_cast(bf16x8, b), c, 0, 0, 0);
}
__device__ __forceinline__ u32x2 tr_read(const char* lds_base, unsigned off) {
  s16x4 v = __builtin_amdgcn_ds_read_tr16_b64_v4i16((LAS s16x4*)(lds_base + off));
  return __builtin_bit_cast(u32x2, v);
}
__device__ __forceinline__ void glds16(const void* g, char* l) {
  __builtin_amdgcn_global_load_lds((const GAS unsigned*)g, (LAS unsigned*)l, 16, 0, 0);
}
__device__ __forceinline__ void glds16_asm(const void* g, unsigned lds_dst) {
  unsigned keep;
  asm volatile("s_mov_b32 %0, m0\n\ts_mov_b32 m0, %2\n\ts_nop 0\n\tglobal_load_lds_dwordx4 %1, off\n\ts_mov_b32 m0, %0"
               : "=&s"(keep) : "v"(g), "s"(lds_dst) : "memory");
}
__device__ __forceinline__ u32x2 tr_read_a(unsigned addr) {
  s16x4 v = __builtin_amdgcn_ds_read_tr16_b64_v4i16((LAS s16x4*)addr);
  return __builtin_bit_cast(u32x2, v);
}
__device__ __forceinline__ unsigned kv_pos(unsigned g, unsigned alpha, unsigned c) {
  unsigned f = ((g & 1u) << 1) | ((alpha & 1u) ^ (g >> 1));
  return 4u * f + c;
}
__device__ __forceinline__ void kstep_tiles(int ks, int& fA, int& fB) {
  if (ks < 45) { int ft = ks / 3, p = ks - 3 * ft; fA = 112 * ft + 32 * p; fB = fA + 16; }
  else if (ks == 45) { fA = 1680; fB = 1696; }
  else { int e = ks - 46; fA = 224 * e + 96; fB = (e < 7) ? (224 * e + 208) : 1712; }
}

#define ST2_DECL
#define ST2(i)
#define ST2_RT(i)
#define ST2_OUT()
#define STAMP(a, b)
#define STAMP_RT(a, b)
#define STAMP_OUT(a, b)
#define DBG_LDS 0
constexpr int NSTG = 3;
constexpr int STGW = 64 * 448;
constexpr int STGB = STGW + 128 * 128;
constexpr int K1_SIDE = NSTG * STGB;
constexpr int K1_CS = K1_SIDE + 20480;
constexpr int K1_DBG = K1_CS + 1024;
constexpr int K1_LDS = K1_DBG + DBG_LDS;

#define SBAR() __builtin_amdgcn_sched_barrier(0)
#define BARRIER() do { asm volatile("" ::: "memory"); __builtin_amdgcn_s_barrier(); asm volatile("" ::: "memory"); } while (0)

__device__ __forceinline__ void glds16_saddr(unsigned voff, const void* sbase, unsigned lds_dst) {
  unsigned keep;
  asm volatile("s_mov_b32 %0, m0\n\ts_mov_b32 m0, %3\n\ts_nop 0\n\tglobal_load_lds_dwordx4 %1, %2\n\ts_mov_b32 m0, %0"
               : "=&s"(keep) : "v"(voff), "s"(sbase), "s"(lds_dst) : "memory");
}
__device__ __forceinline__ u32x4 lds_rd128(unsigned addr) { return *(const LAS u32x4*)addr; }
__device__ __forceinline__ f32x16 mfma32(u32x4 a, u32x4 b, f32x16 c) {
  return __builtin_amdgcn_mfma_f32_32x32x16_bf16(__builtin_bit_cast(bf16x8, a), __builtin_bit_cast(bf16x8, b), c, 0, 0, 0);
}

__global__ __launch_bounds__(512, 2) void k1_kernel(const u16* __restrict__ x, const u16* __restrict__ kin,
                                                    const u16* __restrict__ vin, const u16* __restrict__ shk,
                                                    const u16* __restrict__ wg, const u16* __restrict__ wu,
                                                    const u16* __restrict__ wd, char* __restrict__ ws) {
  extern __shared__ __attribute__((aligned(16))) char smem[];
  const int b = blockIdx.x;
  const int w = __builtin_amdgcn_readfirstlane(threadIdx.x >> 6);
  const int lane = threadIdx.x & 63;
  const unsigned sbase = (unsigned)(size_t)(LAS char*)smem;
  const int xcd = b & 7, bi = b >> 3;
  const int mt = (xcd >> 2) * 8 + (bi >> 2);
  const int ft = (xcd & 3) * 4 + (bi & 3);
  STAMP_RT(K1_DBG, 0);
  STAMP(K1_DBG, 1);

  if (w >= 6) {
    const int alpha = b >> 4, cg = b & 15;
    const int u = lane >> 5, m = lane & 31;
    const int s0 = alpha * 128 + 4 * m;
    if (w == 7 && b == 0 && lane < 8) {
      __hip_atomic_store((GAS unsigned*)(ws + WS_CNT + lane * 256), 0u, __ATOMIC_RELAXED, __HIP_MEMORY_SCOPE_AGENT);
    }
    BARRIER();
#pragma unroll 1
    for (int pass = 0; pass < 8 - w; ++pass) {
      const int cc = pass ? 4 : (w - 6) * 2 + u;
      const int d0 = cg * 40 + cc * 8;
      u32x4 kr[7], vr[4], sw[4];
#pragma unroll
      for (int i = 0; i < 7; ++i) {
        int s = s0 - 3 + i;
        u32x4 t = *(const u32x4*)(kin + (size_t)max(s, 0) * D_ + d0);
        u32x4 z = {0u, 0u, 0u, 0u};
        kr[i] = (s >= 0) ? t : z;
      }
#pragma unroll
      for (int i = 0; i < 4; ++i) vr[i] = *(const u32x4*)(vin + (size_t)(s0 + i) * D_ + d0);
#pragma unroll
      for (int i = 0; i < 4; ++i) sw[i] = *(const u32x4*)(shk + (size_t)d0 * 4 + i * 8);
      char* kb = smem + K1_SIDE + cc * 2048;
      const unsigned wb = (unsigned)((m & 7) * 2);
      BARRIER();
#pragma unroll
      for (int e = 0; e < 8; ++e) {
        if ((e & 3) == 0) BARRIER();
        unsigned t01 = sw[e >> 1][(e & 1) * 2], t23 = sw[e >> 1][(e & 1) * 2 + 1];
        float w0 = bf_lo(t01), w1 = bf_hi(t01), w2 = bf_lo(t23), w3 = bf_hi(t23);
        float kf[7];
#pragma unroll
        for (int i = 0; i < 7; ++i) { unsigned t = kr[i][e >> 1]; kf[i] = (e & 1) ? bf_hi(t) : bf_lo(t); }
#pragma unroll
        for (int c = 0; c < 4; ++c) {
          unsigned t = vr[c][e >> 1];
          float vv = (e & 1) ? bf_hi(t) : bf_lo(t);
          float a = kf[c] * w3;
          a = fmaf(kf[c + 1], w2, a);
          a = fmaf(kf[c + 2], w1, a);
          a = bf16r(fmaf(kf[c + 3], w0, a));
          __bf16 r = (__bf16)(a * vv);
          *(LAS u16*)(kb + e * 256 + kv_pos(m >> 3, alpha, c) * 16 + wb) = __builtin_bit_cast(u16, r);
        }
      }
      asm volatile("s_waitcnt lgkmcnt(0)" ::: "memory");
      {
        const int ch0 = pass ? 32 : (w - 6) * 16;
#pragma unroll
        for (int i = 0; i < 4; ++i) {
          if (i < 2 || pass == 0) {
            int idx = i * 64 + lane;
            int chl = ch0 + (idx >> 4);
            u32x4 v = *(LAS u32x4*)(smem + K1_SIDE + chl * 256 + (idx & 15) * 16);
            *(u32x4*)(ws + WS_KVP + ((size_t)(cg * 40 + chl) * 16 + alpha) * 256 + (idx & 15) * 16) = v;
          }
        }
      }
    }
    if (w == 7) {
      u32x4 xr[10];
      {
        const u16* xp = x + (size_t)(b * 8 + (lane >> 3)) * D_ + (lane & 7) * 8;
#pragma unroll
        for (int i = 0; i < 10; ++i) xr[i] = *(const u32x4*)(xp + i * 64);
      }
      const bool dowd = b < 216;
      const int ks = b >> 2, nq = b & 3;
      char* lt = smem + K1_SIDE + 10240;
      if (dowd) {
        int fA, fB;
        kstep_tiles(ks, fA, fB);
#pragma unroll
        for (int i = 0; i < 10; ++i) {
          int idx = i * 64 + lane;
          int r = idx / 20, ch = idx - r * 20;
          int f = (r < 16) ? (fA + r) : (fB + r - 16);
          glds16_asm(wd + (size_t)f * D_ + nq * 160 + ch * 8, (unsigned)(size_t)(LAS char*)(lt + i * 1024));
        }
      }
      BARRIER();
      BARRIER();
      asm volatile("s_waitcnt vmcnt(0)" ::: "memory");
      if (dowd) {
        const int g = lane >> 4, q = (lane & 15) >> 2, p = lane & 3;
        const unsigned rb = (unsigned)((16 * (g >> 1) + 4 * (g & 1) + q) * 320 + p * 8);
#pragma unroll
        for (int n = 0; n < 10; ++n) {
          u32x2 lo = tr_read(lt, rb + n * 32);
          u32x2 hi = tr_read(lt, rb + n * 32 + 8 * 320);
          u32x4 v = {lo.x, lo.y, hi.x, hi.y};
          *(u32x4*)(ws + WS_WDT + ((size_t)((nq * 10 + n) * NKS2 + ks) * 64 + lane) * 16) = v;
        }
      }
      float sm = 0.f;
#pragma unroll
      for (int i = 0; i < 10; ++i)
#pragma unroll
        for (int e = 0; e < 4; ++e) sm += bf_lo(xr[i][e]) + bf_hi(xr[i][e]);
      sm += __shfl_xor(sm, 1);
      sm += __shfl_xor(sm, 2);
      sm += __shfl_xor(sm, 4);
      const float mean = sm * (1.0f / D_);
      BARRIER();
      float sq = 0.f;
#pragma unroll
      for (int i = 0; i < 10; ++i)
#pragma unroll
        for (int e = 0; e < 4; ++e) {
          float d0 = bf_lo(xr[i][e]) - mean, d1 = bf_hi(xr[i][e]) - mean;
          sq = fmaf(d0, d0, sq);
          sq = fmaf(d1, d1, sq);
        }
      sq += __shfl_xor(sq, 1);
      sq += __shfl_xor(sq, 2);
      sq += __shfl_xor(sq, 4);
      const float var = sq * (1.0f / D_);
      if ((lane & 7) == 0) {
        f32x2 st = {bf16r(mean), bf16r(sqrtf(bf16r(bf16r(var) + 1e-5f)))};
        *(f32x2*)(ws + WS_STATS + (size_t)(b * 8 + (lane >> 3)) * 8) = st;
      }
    }
    STAMP(K1_DBG, 2);
    STAMP_RT(K1_DBG, 31);
    STAMP_OUT(K1_DBG, 0);
    return;
  }

  if (w >= 4) {
    const int lw = w - 4;
    const char* wsrc;
    {
      int r = lane >= 28, ch = lane - 28 * r;
      int mat = ch >= 14;
      int col = min(ft * 112 + (ch - 14 * mat) * 8, F_ - 8);
      wsrc = (const char*)((mat ? wu : wg) + (size_t)(lw * 32 + r) * F_ + col);
    }
    const unsigned xo0 = (unsigned)((lane >> 3) * (D_ * 2) + (((lane & 7) ^ (lane >> 4)) * 16));
    const unsigned xo1 = (unsigned)((lane >> 3) * (D_ * 2) + (((lane & 7) ^ (lane >> 4) ^ 4) * 16));
    const char* xs = (const char*)(x + (size_t)(mt * 128 + lw * 64) * D_);
    unsigned lws = sbase;
#pragma unroll 1
    for (int s = -2; s < 10; ++s) {
      if (s >= 0) {
        if (s < 9) asm volatile("s_waitcnt vmcnt(24)" ::: "memory");
        else asm volatile("s_waitcnt vmcnt(0)" ::: "memory");
        BARRIER();
      }
      if (s < 8) {
#pragma unroll
        for (int i = 0; i < 4; ++i) {
          glds16_saddr(xo0, xs + i * (16 * D_ * 2), lws + (unsigned)(STGW + (lw * 8 + 2 * i) * 1024));
          glds16_saddr(xo1, xs + i * (16 * D_ * 2) + 8 * D_ * 2, lws + (unsigned)(STGW + (lw * 8 + 2 * i + 1) * 1024));
        }
        if (lane < 56) {
          const char* ptr = wsrc;
          unsigned dst = lws + (unsigned)(lw * 16 * 896);
#pragma unroll 4
          for (int i = 0; i < 16; ++i) {
            glds16_asm(ptr, dst);
            ptr += 2 * F_ * 2;
            dst += 896;
          }
        }
        wsrc += 64 * F_ * 2;
        xs += 128;
        lws = (lws == sbase + 2 * STGB) ? sbase : lws + STGB;
      }
    }
    STAMP(K1_DBG, 2);
    STAMP_RT(K1_DBG, 31);
    STAMP_OUT(K1_DBG, 0);
    return;
  }

  const int hi = lane >> 5, q = (lane & 15) >> 2, p = lane & 3;
  int tj[7];
  unsigned roff[7];
#pragma unroll
  for (int j = 0; j < 7; ++j) {
    int jj = j;
    if (w < 3) {
      if (j < 2) jj = 2 * w + j;
      else if ((j >> 1) == w) jj = j & 1;
    } else {
      if (j == 0) jj = 6;
      else if (j == 6) jj = 0;
    }
    tj[j] = jj;
    roff[j] = sbase + (unsigned)((8 * hi + q) * 448 + ((lane >> 4) & 1) * 224 + p * 8 + jj * 32);
  }
  unsigned aoff[4];
#pragma unroll
  for (int kq = 0; kq < 4; ++kq)
    aoff[kq] = sbase + (unsigned)(STGW + (w * 32 + (lane & 31)) * 128 + (((2 * kq + hi) ^ ((lane >> 1) & 7)) * 16));

  f32x16 acc[7];
#pragma unroll
  for (int c = 0; c < 7; ++c)
#pragma unroll
    for (int e = 0; e < 16; ++e) acc[c][e] = 0.f;
  f32x4 accs = {0.f, 0.f, 0.f, 0.f}, accc[2] = {{0.f, 0.f, 0.f, 0.f}, {0.f, 0.f, 0.f, 0.f}};
  const unsigned sv = ((lane & 15) == ((lane >> 4) & 1)) ? 0x3f803f80u : 0u;
  u32x4 sel = {sv, sv, sv, sv};
  asm volatile("" : "+v"(sel));
  u32x4 cw[2] = {{0u, 0u, 0u, 0u}, {0u, 0u, 0u, 0u}}, cx = {0u, 0u, 0u, 0u};
  STAMP(K1_DBG, 2);

  int slot = 0;
#pragma unroll 1
  for (int s = 0; s < 10; ++s) {
    STAMP(K1_DBG, 3 + 2 * s);
    asm volatile("s_waitcnt lgkmcnt(0)" ::: "memory");
    BARRIER();
    STAMP(K1_DBG, 4 + 2 * s);
    u32x4 xa[4], wf[28];
    auto rd = [&](int t) __attribute__((always_inline)) {
      const int kq = t / 7, c = t % 7;
      u32x2 lo = tr_read_a(roff[c] + kq * 16 * 448);
      u32x2 hh = tr_read_a(roff[c] + kq * 16 * 448 + 4 * 448);
      wf[t] = (u32x4){lo.x, lo.y, hh.x, hh.y};
    };
    xa[0] = lds_rd128(aoff[0]);
    rd(0);
    rd(1);
    rd(2);
    rd(3);
    SBAR();
    acc[5] = mfma32(cw[0], cx, acc[5]);
    acc[6] = mfma32(cw[1], cx, acc[6]);
    SBAR();
#pragma unroll
    for (int t = 0; t < 26; ++t) {
      const int kq = t / 7, c = t % 7;
      if (t + 4 < 28) rd(t + 4);
      if (c == 2 && kq < 3) xa[kq + 1] = lds_rd128(aoff[kq + 1]);
      SBAR();
      acc[c] = mfma32(wf[t], xa[kq], acc[c]);
      if (c < 2) accc[c] = mfma16(wf[t], sel, accc[c]);
      if (c == 2) accs = mfma16(sel, xa[kq], accs);
      SBAR();
    }
    cw[0] = wf[26];
    cw[1] = wf[27];
    cx = xa[3];
    const unsigned dl = (slot == 2) ? (unsigned)(-2 * STGB) : (unsigned)STGB;
    slot = (slot == 2) ? 0 : slot + 1;
#pragma unroll
    for (int c = 0; c < 7; ++c) roff[c] += dl;
#pragma unroll
    for (int c = 0; c < 4; ++c) aoff[c] += dl;
  }
  acc[5] = mfma32(cw[0], cx, acc[5]);
  acc[6] = mfma32(cw[1], cx, acc[6]);

  STAMP(K1_DBG, 23);
  float* csb = (float*)(smem + K1_CS);
  if ((lane & 15) < 2) {
    float* cp = csb + (lane & 15) * 112 + 4 * (lane >> 4);
    *(f32x4*)(cp + tj[0] * 16) = accc[0];
    if (w < 3) *(f32x4*)(cp + tj[1] * 16) = accc[1];
  }
  float nmb;
  {
    float v0 = __shfl(accs[0], lane & 15), v1 = __shfl(accs[1], lane & 15);
    nmb = -bf16r(((lane & 16) ? v1 : v0) * (1.0f / D_));
  }
  asm volatile("s_waitcnt lgkmcnt(0)" ::: "memory");
  BARRIER();
  char* hb = ws + WS_HT + ((size_t)((mt * 8 + w * 2 + ((lane >> 4) & 1)) * NKS2) * 64 + (lane & 15) + 16 * hi) * 16;
  const f32x2 nm2 = {nmb, nmb};
#pragma unroll
  for (int j = 0; j < 7; ++j) {
    const int jj = tj[j];
    const float* cp = csb + jj * 16 + 4 * hi;
    f32x4 c4[4] = {*(const f32x4*)(cp), *(const f32x4*)(cp + 8), *(const f32x4*)(cp + 112), *(const f32x4*)(cp + 120)};
    u32x4 o;
#pragma unroll
    for (int i = 0; i < 4; ++i) {
      f32x2 gv = {acc[j][2 * i], acc[j][2 * i + 1]}, uv = {acc[j][8 + 2 * i], acc[j][9 + 2 * i]};
      f32x2 cgv = {c4[i >> 1][(2 * i) & 3], c4[i >> 1][(2 * i + 1) & 3]};
      f32x2 cuv = {c4[2 + (i >> 1)][(2 * i) & 3], c4[2 + (i >> 1)][(2 * i + 1) & 3]};
      gv = __builtin_elementwise_fma(nm2, cgv, gv);
      uv = __builtin_elementwise_fma(nm2, cuv, uv);
      f32x2 hv = gv * uv;
      o[i] = pack_bf16(hv.x, hv.y);
    }
    int ks = 3 * ft + (jj >> 1), tb = jj & 1;
    if (jj == 6) { ks = 46 + (ft >> 1); tb = ft & 1; }
    if (ft == 15 && jj == 2) { ks = 53; tb = 1; }
    if (ft < 15 || jj < 3) *(u32x4*)(hb + (size_t)ks * 1024 + tb * 512) = o;
  }
  STAMP(K1_DBG, 24);
  STAMP_RT(K1_DBG, 31);
  STAMP_OUT(K1_DBG, 0);
}

constexpr int K2_LDS = 4 * 20 * 1024;
constexpr int NCONV = 160;

template <int OFF>
__device__ __forceinline__ void gload16s(u32x4& dst, unsigned voff, const void* sbase) {
  asm volatile("global_load_dwordx4 %0, %1, %2 offset:%3" : "=v"(dst) : "v"(voff), "s"(sbase), "i"(OFF) : "memory");
}
template <int OFF>
__device__ __forceinline__ void gload8s(u32x2& dst, unsigned voff, const void* sbase) {
  asm volatile("global_load_dwordx2 %0, %1, %2 offset:%3" : "=v"(dst) : "v"(voff), "s"(sbase), "i"(OFF) : "memory");
}
template <int N>
__device__ __forceinline__ float dpp_shr(float v) {
  return __builtin_bit_cast(float, __builtin_amdgcn_update_dpp(0, __builtin_bit_cast(int, v), 0x110 + N, 0xf, 0xf, false));
}
template <int N>
__device__ __forceinline__ float dpp_shl(float v) {
  return __builtin_bit_cast(float, __builtin_amdgcn_update_dpp(0, __builtin_bit_cast(int, v), 0x100 + N, 0xf, 0xf, false));
}

__global__ __launch_bounds__(256, 2) void k2_kernel(const u16* __restrict__ x, const u16* __restrict__ ssmk,
                                                    const u16* __restrict__ ssmd, char* ws, u16* __restrict__ out) {
  extern __shared__ __attribute__((aligned(16))) char smem[];
  const int w = __builtin_amdgcn_readfirstlane(threadIdx.x >> 6);
  const int lane = threadIdx.x & 63;
  const int g = lane >> 4;
  ST2_DECL;
  ST2_RT(0);
  ST2(1);

  if (blockIdx.x < NCONV) {
    const int cb = blockIdx.x;
    const int d = cb * 4 + w;
    char* lb = smem + w * 9216;
    char* Kd = lb + 256;
    char* Xd = lb + 5120;
#pragma unroll
    for (int i = 0; i < 4; ++i) {
      glds16(ssmk + (size_t)d * S_ + i * 512 + lane * 8, Kd + i * 1024);
      glds16(ws + WS_KVP + (size_t)d * 4096 + i * 1024 + lane * 16, Xd + i * 1024);
    }
    {
      u32x4 z = {0u, 0u, 0u, 0u};
      *(LAS u32x4*)(lb + ((lane < 16) ? lane * 16 : 4352 - 256 + lane * 16)) = z;
    }
    const float dd = bf_lo((unsigned)ssmd[d]);
    asm volatile("s_waitcnt vmcnt(0) lgkmcnt(0)" ::: "memory");
    ST2(2);

    const int q = (lane & 15) >> 2, p = lane & 3;
    const unsigned kof = (unsigned)(256 - 64 * g - 8 * q + 8 * p);
    const int al = (lane >> 2) & 3, c = lane & 3;
    const unsigned xo0 = (unsigned)(5120 + 256 * al + 16 * kv_pos(g, al, c));
    const unsigned xo1 = (unsigned)(5120 + 256 * al + 16 * kv_pos(g, al + 1, c));

    f32x4 acc[8][4];
#pragma unroll
    for (int t = 0; t < 8; ++t)
#pragma unroll
      for (int G = 0; G < 4; ++G) acc[t][G] = (f32x4){0.f, 0.f, 0.f, 0.f};

#pragma unroll
    for (int ph = 0; ph < 4; ++ph) {
#pragma unroll 1
      for (int mh = 0; mh < 2; ++mh) {
#pragma unroll
        for (int mo = 0; mo < 2; ++mo) {
          const int mu = ph * 4 + mh * 2 + mo;
          u32x4 xf[4];
#pragma unroll
          for (int G = ph; G < 4; ++G)
            xf[G] = *(LAS u32x4*)(lb + (mo ? xo1 : xo0) + 256 * (4 * G - mu));
#pragma unroll
          for (int t = 0; t < 8; ++t) {
            u32x2 lo = tr_read(lb, kof + 256 * mu + 32 * t);
            u32x2 hi = tr_read(lb, kof + 256 * mu + 32 * t - 32);
            u32x4 kf = {lo.x, lo.y, hi.x, hi.y};
#pragma unroll
            for (int G = ph; G < 4; ++G) acc[t][G] = mfma16(xf[G], kf, acc[t][G]);
          }
        }
      }
    }
    ST2(3);
    const int i16 = lane & 15;
    u16 yo[8][4];
#pragma unroll
    for (int G = 0; G < 4; ++G) {
      float pv[4];
#pragma unroll
      for (int r = 1; r < 4; ++r) {
        float prov = (g == 3) ? ((G > 0) ? acc[7][G > 0 ? G - 1 : 0][r] : 0.f) : acc[7][G][r];
        pv[r] = __shfl(prov, (lane + 48) & 63);
      }
#pragma unroll
      for (int t = 0; t < 8; ++t) {
        float y = acc[t][G][0] + dpp_shr<1>(acc[t][G][1]) + dpp_shr<2>(acc[t][G][2]) + dpp_shr<3>(acc[t][G][3]);
        float p1 = (t > 0) ? acc[t > 0 ? t - 1 : 0][G][1] : pv[1];
        float p2 = (t > 0) ? acc[t > 0 ? t - 1 : 0][G][2] : pv[2];
        float p3 = (t > 0) ? acc[t > 0 ? t - 1 : 0][G][3] : pv[3];
        y += dpp_shl<15>(p1) + dpp_shl<14>(p2) + dpp_shl<13>(p3);
        const unsigned ao = (unsigned)(4 * G + g);
        unsigned kvo = 5120 + 256 * ao + 16 * kv_pos((unsigned)(t >> 1), ao, (unsigned)(i16 & 3)) + 2 * (4 * (t & 1) + (i16 >> 2));
        float kvv = bf_lo((unsigned)*(LAS u16*)(lb + kvo));
        __bf16 r = (__bf16)(bf16r(y) + bf16r(kvv * dd));
        yo[t][G] = __builtin_bit_cast(u16, r);
      }
    }
    ST2(4);
    __syncthreads();
#pragma unroll
    for (int G = 0; G < 4; ++G)
#pragma unroll
      for (int t = 0; t < 8; ++t) {
        int s = 128 * (4 * G + g) + 16 * t + i16;
        *(LAS u16*)(smem + s * 8 + w * 2) = yo[t][G];
      }
    __syncthreads();
#pragma unroll
    for (int i = 0; i < 8; ++i) {
      int s = threadIdx.x + 256 * i;
      unsigned long long v = *(LAS unsigned long long*)(smem + s * 8);
      size_t o = WS_YT + ((size_t)((s >> 4) * 40 + (cb >> 2)) * 64 + (s & 15) + 16 * (cb & 3)) * 8;
      __hip_atomic_store((GAS unsigned long long*)(ws + o), v, __ATOMIC_RELAXED, __HIP_MEMORY_SCOPE_AGENT);
    }
    ST2(5);
    asm volatile("s_waitcnt vmcnt(0)" ::: "memory");
    __syncthreads();
    if (threadIdx.x == 0)
      __hip_atomic_fetch_add((GAS unsigned*)(ws + WS_CNT + (cb / 20) * 256), 1u, __ATOMIC_RELAXED, __HIP_MEMORY_SCOPE_AGENT);
    ST2(6);
    ST2_RT(11);
    ST2_OUT();
    return;
  }

  const int b = blockIdx.x - NCONV;
  const int xcd = b & 7, bi = b >> 3;
  const int mt = (xcd >> 1) * 8 + (bi >> 2);
  const int nt = (xcd & 1) * 4 + (bi & 3);
  const int ksf = (w < 2) ? w * 14 : 27 + (w - 2) * 13;
  const char* hp = ws + WS_HT + (size_t)(mt * 4 * NKS2 + ksf) * 1024;
  const char* wp = ws + WS_WDT + (size_t)(nt * 5 * NKS2 + ksf) * 1024;
  const int m16 = mt * 4 + w;
  const int row = m16 * 16 + (lane & 15);

  u32x2 xv[5], stv;
  {
    const unsigned xo = (unsigned)((row * D_ + nt * 80 + 4 * g) * 2);
    gload8s<0>(xv[0], xo, x);
    gload8s<32>(xv[1], xo, x);
    gload8s<64>(xv[2], xo, x);
    gload8s<96>(xv[3], xo, x);
    gload8s<128>(xv[4], xo, x);
    gload8s<0>(stv, (unsigned)(row * 8), ws + WS_STATS);
  }

  f32x4 acc[4][5];
#pragma unroll
  for (int m = 0; m < 4; ++m)
#pragma unroll
    for (int n = 0; n < 5; ++n) acc[m][n] = (f32x4){0.f, 0.f, 0.f, 0.f};

  u32x4 hf[4][4], wf[4][5];
  unsigned vo[4];
#pragma unroll
  for (int i = 0; i < 4; ++i) vo[i] = (unsigned)(lane * 16 + i * 4096);
#define LOADK(j)                                                                           \
  do {                                                                                     \
    _Pragma("unroll") for (int m = 0; m < 4; ++m)                                          \
        gload16s<((j) & 3) * 1024>(hf[(j) & 3][m], vo[(j) >> 2], hp + (size_t)m * NKS2 * 1024); \
    _Pragma("unroll") for (int n = 0; n < 5; ++n)                                          \
        gload16s<((j) & 3) * 1024>(wf[(j) & 3][n], vo[(j) >> 2], wp + (size_t)n * NKS2 * 1024); \
  } while (0)
#define WAITK(j, N)                                                                        \
  asm volatile("s_waitcnt vmcnt(" #N ")"                                                   \
               : "+v"(hf[(j) & 3][0]), "+v"(hf[(j) & 3][1]), "+v"(hf[(j) & 3][2]), "+v"(hf[(j) & 3][3]), \
                 "+v"(wf[(j) & 3][0]), "+v"(wf[(j) & 3][1]), "+v"(wf[(j) & 3][2]), "+v"(wf[(j) & 3][3]), \
                 "+v"(wf[(j) & 3][4])::"memory")
#define MMAK(j)                                                                            \
  do {                                                                                     \
    _Pragma("unroll") for (int m = 0; m < 4; ++m)                                          \
        _Pragma("unroll") for (int n = 0; n < 5; ++n)                                      \
            acc[m][n] = mfma16(wf[(j) & 3][n], hf[(j) & 3][m], acc[m][n]);                 \
    SBAR();                                                                                \
  } while (0)
  if (w < 2) LOADK(0);
  LOADK(1);
  LOADK(2);
  LOADK(3);
  if (w < 2) { WAITK(0, 27); MMAK(0); }
  LOADK(4);  WAITK(1, 27);  MMAK(1);
  LOADK(5);  WAITK(2, 27);  MMAK(2);
  LOADK(6);  WAITK(3, 27);  MMAK(3);
  LOADK(7);  WAITK(4, 27);  MMAK(4);
  LOADK(8);  WAITK(5, 27);  MMAK(5);
  LOADK(9);  WAITK(6, 27);  MMAK(6);
  LOADK(10); WAITK(7, 27);  MMAK(7);
  LOADK(11); WAITK(8, 27);  MMAK(8);
  LOADK(12); WAITK(9, 27);  MMAK(9);
  LOADK(13); WAITK(10, 27); MMAK(10);
  WAITK(11, 18); MMAK(11);
  WAITK(12, 9);  MMAK(12);
  WAITK(13, 0);  MMAK(13);
  asm volatile("" : "+v"(xv[0]), "+v"(xv[1]), "+v"(xv[2]), "+v"(xv[3]), "+v"(xv[4]), "+v"(stv));
  ST2(3);
#pragma unroll
  for (int m = 0; m < 4; ++m)
#pragma unroll
    for (int n = 0; n < 5; ++n) *(LAS f32x4*)(smem + ((w * 20 + m * 5 + n) * 64 + lane) * 16) = acc[m][n];
  {
    GAS unsigned* cnt = (GAS unsigned*)(ws + WS_CNT + nt * 256);
    while (__hip_atomic_load(cnt, __ATOMIC_RELAXED, __HIP_MEMORY_SCOPE_AGENT) < 20u) __builtin_amdgcn_s_sleep(2);
  }
  ST2(4);
  unsigned long long yv[5];
#pragma unroll
  for (int n = 0; n < 5; ++n)
    yv[n] = __hip_atomic_load((GAS unsigned long long*)(ws + WS_YT + ((size_t)(m16 * 40 + nt * 5 + n) * 64 + lane) * 8),
                              __ATOMIC_RELAXED, __HIP_MEMORY_SCOPE_AGENT);
  asm volatile("s_waitcnt lgkmcnt(0)" ::: "memory");
  __builtin_amdgcn_s_barrier();
  asm volatile("" ::: "memory");
  ST2(5);
  const float mean = __uint_as_float(stv.x), den = __uint_as_float(stv.y);
  const float rs2 = 1.0f / (den * den);
#pragma unroll
  for (int n = 0; n < 5; ++n) {
    f32x4 s = *(LAS f32x4*)(smem + ((0 * 20 + w * 5 + n) * 64 + lane) * 16);
#pragma unroll
    for (int ww = 1; ww < 4; ++ww) s += *(LAS f32x4*)(smem + ((ww * 20 + w * 5 + n) * 64 + lane) * 16);
    const int col = (nt * 5 + n) * 16 + 4 * g;
    unsigned y0 = (unsigned)yv[n], y1 = (unsigned)(yv[n] >> 32);
    float xs[4] = {bf_lo(xv[n].x), bf_hi(xv[n].x), bf_lo(xv[n].y), bf_hi(xv[n].y)};
    float ys[4] = {bf_lo(y0), bf_hi(y0), bf_lo(y1), bf_hi(y1)};
    float o[4];
#pragma unroll
    for (int e = 0; e < 4; ++e) {
      float xn = bf16r(bf16r(xs[e] - mean) / den);
      float t = bf16r(xs[e] + bf16r(xn * ys[e]));
      o[e] = t + bf16r(s[e] * rs2);
    }
    u32x2 ov = {pack_bf16(o[0], o[1]), pack_bf16(o[2], o[3])};
    *(u32x2*)(out + (size_t)row * D_ + col) = ov;
  }
  ST2(6);
  ST2_RT(11);
  ST2_OUT();
}

extern "C" void kernel_launch(void* const* d_in, const int* in_sizes, int n_in, void* d_out, int out_size, void* d_ws,
                              size_t ws_size, hipStream_t stream) {
  const u16* q = (const u16*)d_in[0];
  const u16* k = (const u16*)d_in[1];
  const u16* v = (const u16*)d_in[2];
  const u16* shk = (const u16*)d_in[3];
  const u16* ssmk = (const u16*)d_in[4];
  const u16* ssmd = (const u16*)d_in[5];
  const u16* wg = (const u16*)d_in[7];
  const u16* wu = (const u16*)d_in[8];
  const u16* wd = (const u16*)d_in[9];
  (void)hipFuncSetAttribute((const void*)k1_kernel, hipFuncAttributeMaxDynamicSharedMemorySize, K1_LDS);
  (void)hipFuncSetAttribute((const void*)k2_kernel, hipFuncAttributeMaxDynamicSharedMemorySize, K2_LDS);
  k1_kernel<<<256, 512, K1_LDS, stream>>>(q, k, v, shk, wg, wu, wd, (char*)d_ws);
  k2_kernel<<<NCONV + 256, 256, K2_LDS, stream>>>(q, ssmk, ssmd, (char*)d_ws, (u16*)d_out);
}
```

```cpp
#include <hip/hip_runtime.h>
#include <hip/hip_bf16.h>
#include <stdint.h>

typedef __bf16 bf16x8 __attribute__((ext_vector_type(8)));
typedef __bf16 bf16x2 __attribute__((ext_vector_type(2)));
typedef short s16x4 __attribute__((ext_vector_type(4)));
typedef float f32x4 __attribute__((ext_vector_type(4)));
typedef float f32x16 __attribute__((ext_vector_type(16)));
typedef float f32x2 __attribute__((ext_vector_type(2)));
typedef unsigned u32x4 __attribute__((ext_vector_type(4)));
typedef unsigned u32x2 __attribute__((ext_vector_type(2)));
typedef unsigned short u16;
#define LAS __attribute__((address_space(3)))
#define GAS __attribute__((address_space(1)))

constexpr int S_ = 2048, D_ = 640, F_ = 1728;
constexpr int NKS2 = 54;

constexpr size_t WS_HT = 0;
constexpr size_t WS_WDT = WS_HT + (size_t)128 * NKS2 * 1024;
constexpr size_t WS_KVP = WS_WDT + (size_t)40 * NKS2 * 1024;
constexpr size_t WS_YT = WS_KVP + (size_t)D_ * 4096;
constexpr size_t WS_STATS = WS_YT + (size_t)S_ * D_ * 2;

__device__ __forceinline__ float bf16r(float x) { return (float)(__bf16)x; }
__device__ __forceinline__ unsigned pack_bf16(float lo, float hi) {
  f32x2 f = {lo, hi};
  return __builtin_bit_cast(unsigned, __builtin_convertvector(f, bf16x2));
}
__device__ __forceinline__ float bf_lo(unsigned u) { return __uint_as_float(u << 16); }
__device__ __forceinline__ float bf_hi(unsigned u) { return __uint_as_float(u & 0xffff0000u); }
__device__ __forceinline__ float dot2(unsigned a, unsigned b, float c) {
  return __builtin_amdgcn_fdot2_f32_bf16(__builtin_bit_cast(bf16x2, a), __builtin_bit_cast(bf16x2, b), c, false);
}
__device__ __forceinline__ f32x4 mfma16(u32x4 a, u32x4 b, f32x4 c) {
  return __builtin_amdgcn_mfma_f32_16x16x32_bf16(__builtin_bit_cast(bf16x8, a), __builtin_bit_cast(bf16x8, b), c, 0, 0, 0);
}
__device__ __forceinline__ u32x2 tr_read(const char* lds_base, unsigned off) {
  s16x4 v = __builtin_amdgcn_ds_read_tr16_b64_v4i16((LAS s16x4*)(lds_base + off));
  return __builtin_bit_cast(u32x2, v);
}
__device__ __forceinline__ void glds16(const void* g, char* l) {
  __builtin_amdgcn_global_load_lds((const GAS unsigned*)g, (LAS unsigned*)l, 16, 0, 0);
}
__device__ __forceinline__ void glds16_asm(const void* g, unsigned lds_dst) {
  unsigned keep;
  asm volatile("s_mov_b32 %0, m0\n\ts_mov_b32 m0, %2\n\ts_nop 0\n\tglobal_load_lds_dwordx4 %1, off\n\ts_mov_b32 m0, %0"
               : "=&s"(keep) : "v"(g), "s"(lds_dst) : "memory");
}
__device__ __forceinline__ u32x2 tr_read_a(unsigned addr) {
  s16x4 v = __builtin_amdgcn_ds_read_tr16_b64_v4i16((LAS s16x4*)addr);
  return __builtin_bit_cast(u32x2, v);
}
__device__ __forceinline__ unsigned kv_pos(unsigned g, unsigned alpha, unsigned c) {
  unsigned f = ((g & 1u) << 1) | ((alpha & 1u) ^ (g >> 1));
  return 4u * f + c;
}
__device__ __forceinline__ void kstep_tiles(int ks, int& fA, int& fB) {
  if (ks < 45) { int ft = ks / 3, p = ks - 3 * ft; fA = 112 * ft + 32 * p; fB = fA + 16; }
  else if (ks == 45) { fA = 1680; fB = 1696; }
  else { int e = ks - 46; fA = 224 * e + 96; fB = (e < 7) ? (224 * e + 208) : 1712; }
}

#define ST2_DECL
#define ST2(i)
#define ST2_RT(i)
#define ST2_OUT()
#define STAMP(a, b)
#define STAMP_RT(a, b)
#define STAMP_OUT(a, b)
#define DBG_LDS 0
constexpr int NSTG = 3;
constexpr int STGW = 64 * 448;
constexpr int STGB = STGW + 128 * 128;
constexpr int K1_SIDE = NSTG * STGB;
constexpr int K1_CS = K1_SIDE + 20480;
constexpr int K1_FLG = K1_CS + 1024;
constexpr int K1_DBG = K1_FLG + 256;
constexpr int K1_LDS = K1_DBG + DBG_LDS;

#define SBAR() __builtin_amdgcn_sched_barrier(0)
#define BARRIER() do { asm volatile("" ::: "memory"); __builtin_amdgcn_s_barrier(); asm volatile("" ::: "memory"); } while (0)

__device__ __forceinline__ void glds16_saddr(unsigned voff, const void* sbase, unsigned lds_dst) {
  unsigned keep;
  asm volatile("s_mov_b32 %0, m0\n\ts_mov_b32 m0, %3\n\ts_nop 0\n\tglobal_load_lds_dwordx4 %1, %2\n\ts_mov_b32 m0, %0"
               : "=&s"(keep) : "v"(voff), "s"(sbase), "s"(lds_dst) : "memory");
}
__device__ __forceinline__ void lds_signal(unsigned addr) {
  unsigned one = 1u;
  asm volatile("s_mov_b64 exec, 1\n\tds_add_u32 %0, %1\n\ts_mov_b64 exec, -1" ::"v"(addr), "v"(one) : "memory");
}
__device__ __forceinline__ unsigned lds_flag(unsigned addr) { return *(volatile LAS unsigned*)addr; }
__device__ __forceinline__ void lds_wait(unsigned addr, unsigned first, unsigned need) {
  if (__builtin_expect((unsigned)__builtin_amdgcn_readfirstlane(first) < need, 0)) {
    unsigned f;
    do { f = lds_flag(addr); } while ((unsigned)__builtin_amdgcn_readfirstlane(f) < need);
  }
  asm volatile("" ::: "memory");
}
__device__ __forceinline__ u32x4 lds_rd128(unsigned addr) { return *(const LAS u32x4*)addr; }
__device__ __forceinline__ f32x16 mfma32(u32x4 a, u32x4 b, f32x16 c) {
  return __builtin_amdgcn_mfma_f32_32x32x16_bf16(__builtin_bit_cast(bf16x8, a), __builtin_bit_cast(bf16x8, b), c, 0, 0, 0);
}

__global__ __launch_bounds__(512, 2) void k1_kernel(const u16* __restrict__ x, const u16* __restrict__ kin,
                                                    const u16* __restrict__ vin, const u16* __restrict__ shk,
                                                    const u16* __restrict__ wg, const u16* __restrict__ wu,
                                                    const u16* __restrict__ wd, char* __restrict__ ws) {
  extern __shared__ __attribute__((aligned(16))) char smem[];
  const int b = blockIdx.x;
  const int w = __builtin_amdgcn_readfirstlane(threadIdx.x >> 6);
  const int lane = threadIdx.x & 63;
  const unsigned sbase = (unsigned)(size_t)(LAS char*)smem;
  const unsigned flg = sbase + K1_FLG;
  const int xcd = b & 7, bi = b >> 3;
  const int mt = (xcd >> 2) * 8 + (bi >> 2);
  const int ft = (xcd & 3) * 4 + (bi & 3);
  STAMP_RT(K1_DBG, 0);
  STAMP(K1_DBG, 1);

  if (w >= 6) {
    const int alpha = b >> 4, cg = b & 15;
    const int u = lane >> 5, m = lane & 31;
    const int s0 = alpha * 128 + 4 * m;
    if (w == 7) *(LAS unsigned*)(flg + lane * 4) = 0u;
    __builtin_amdgcn_s_waitcnt(0xC07F);
    BARRIER();
#pragma unroll 1
    for (int pass = 0; pass < 8 - w; ++pass) {
      const int cc = pass ? 4 : (w - 6) * 2 + u;
      const int d0 = cg * 40 + cc * 8;
      u32x4 kr[7], vr[4], sw[4];
#pragma unroll
      for (int i = 0; i < 7; ++i) {
        int s = s0 - 3 + i;
        u32x4 t = *(const u32x4*)(kin + (size_t)max(s, 0) * D_ + d0);
        u32x4 z = {0u, 0u, 0u, 0u};
        kr[i] = (s >= 0) ? t : z;
      }
#pragma unroll
      for (int i = 0; i < 4; ++i) vr[i] = *(const u32x4*)(vin + (size_t)(s0 + i) * D_ + d0);
#pragma unroll
      for (int i = 0; i < 4; ++i) sw[i] = *(const u32x4*)(shk + (size_t)d0 * 4 + i * 8);
      char* kb = smem + K1_SIDE + cc * 2048;
      const unsigned wb = (unsigned)((m & 7) * 2);
#pragma unroll
      for (int e = 0; e < 8; ++e) {
        unsigned t01 = sw[e >> 1][(e & 1) * 2], t23 = sw[e >> 1][(e & 1) * 2 + 1];
        float w0 = bf_lo(t01), w1 = bf_hi(t01), w2 = bf_lo(t23), w3 = bf_hi(t23);
        float kf[7];
#pragma unroll
        for (int i = 0; i < 7; ++i) { unsigned t = kr[i][e >> 1]; kf[i] = (e & 1) ? bf_hi(t) : bf_lo(t); }
#pragma unroll
        for (int c = 0; c < 4; ++c) {
          unsigned t = vr[c][e >> 1];
          float vv = (e & 1) ? bf_hi(t) : bf_lo(t);
          float a = kf[c] * w3;
          a = fmaf(kf[c + 1], w2, a);
          a = fmaf(kf[c + 2], w1, a);
          a = bf16r(fmaf(kf[c + 3], w0, a));
          __bf16 r = (__bf16)(a * vv);
          *(LAS u16*)(kb + e * 256 + kv_pos(m >> 3, alpha, c) * 16 + wb) = __builtin_bit_cast(u16, r);
        }
      }
      {
        const int ch0 = pass ? 32 : (w - 6) * 16;
#pragma unroll
        for (int i = 0; i < 4; ++i) {
          if (i < 2 || pass == 0) {
            int idx = i * 64 + lane;
            int chl = ch0 + (idx >> 4);
            u32x4 v = *(LAS u32x4*)(smem + K1_SIDE + chl * 256 + (idx & 15) * 16);
            *(u32x4*)(ws + WS_KVP + ((size_t)(cg * 40 + chl) * 16 + alpha) * 256 + (idx & 15) * 16) = v;
          }
        }
      }
    }
    if (w == 7) {
      u32x4 xr[10];
      {
        const u16* xp = x + (size_t)(b * 8 + (lane >> 3)) * D_ + (lane & 7) * 8;
#pragma unroll
        for (int i = 0; i < 10; ++i) xr[i] = *(const u32x4*)(xp + i * 64);
      }
      const bool dowd = b < 216;
      const int ks = b >> 2, nq = b & 3;
      char* lt = smem + K1_SIDE + 10240;
      if (dowd) {
        int fA, fB;
        kstep_tiles(ks, fA, fB);
#pragma unroll
        for (int i = 0; i < 10; ++i) {
          int idx = i * 64 + lane;
          int r = idx / 20, ch = idx - r * 20;
          int f = (r < 16) ? (fA + r) : (fB + r - 16);
          glds16_asm(wd + (size_t)f * D_ + nq * 160 + ch * 8, (unsigned)(size_t)(LAS char*)(lt + i * 1024));
        }
      }
      asm volatile("s_waitcnt vmcnt(0)" ::: "memory");
      if (dowd) {
        const int g = lane >> 4, q = (lane & 15) >> 2, p = lane & 3;
        const unsigned rb = (unsigned)((16 * (g >> 1) + 4 * (g & 1) + q) * 320 + p * 8);
#pragma unroll
        for (int n = 0; n < 10; ++n) {
          u32x2 lo = tr_read(lt, rb + n * 32);
          u32x2 hi = tr_read(lt, rb + n * 32 + 8 * 320);
          u32x4 v = {lo.x, lo.y, hi.x, hi.y};
          *(u32x4*)(ws + WS_WDT + ((size_t)((nq * 10 + n) * NKS2 + ks) * 64 + lane) * 16) = v;
        }
      }
      float sm = 0.f;
#pragma unroll
      for (int i = 0; i < 10; ++i)
#pragma unroll
        for (int e = 0; e < 4; ++e) sm += bf_lo(xr[i][e]) + bf_hi(xr[i][e]);
      sm += __shfl_xor(sm, 1);
      sm += __shfl_xor(sm, 2);
      sm += __shfl_xor(sm, 4);
      const float mean = sm * (1.0f / D_);
      float sq = 0.f;
#pragma unroll
      for (int i = 0; i < 10; ++i)
#pragma unroll
        for (int e = 0; e < 4; ++e) {
          float d0 = bf_lo(xr[i][e]) - mean, d1 = bf_hi(xr[i][e]) - mean;
          sq = fmaf(d0, d0, sq);
          sq = fmaf(d1, d1, sq);
        }
      sq += __shfl_xor(sq, 1);
      sq += __shfl_xor(sq, 2);
      sq += __shfl_xor(sq, 4);
      const float var = sq * (1.0f / D_);
      if ((lane & 7) == 0) {
        f32x2 st = {bf16r(mean), bf16r(sqrtf(bf16r(bf16r(var) + 1e-5f)))};
        *(f32x2*)(ws + WS_STATS + (size_t)(b * 8 + (lane >> 3)) * 8) = st;
      }
    } else {
      const u32x4 mk = {~0u, ~0u, ~0u, ~0u};
#pragma unroll
      for (int i = 0; i < 10; ++i) *(u32x4*)(ws + WS_YT + (size_t)b * 10240 + i * 1024 + lane * 16) = mk;
    }
    STAMP(K1_DBG, 2);
    STAMP_RT(K1_DBG, 31);
    STAMP_OUT(K1_DBG, 0);
    return;
  }

  const char* wsrc;
  {
    int r = lane >= 28, ch = lane - 28 * r;
    int mat = ch >= 14;
    int col = min(ft * 112 + (ch - 14 * mat) * 8, F_ - 8);
    wsrc = (const char*)((mat ? wu : wg) + (size_t)r * F_ + col);
  }
  const unsigned xo0 = (unsigned)((lane >> 3) * (D_ * 2) + (((lane & 7) ^ (lane >> 4)) * 16));
  const unsigned xo1 = xo0 ^ 64u;
  const char* xs = (const char*)(x + (size_t)(mt * 128) * D_);

  if (w >= 4) {
    const int lw = w - 4;
    wsrc += (size_t)(64 + lw * 32) * F_ * 2;
    xs += lw * 64 * D_ * 2 + 128;
    unsigned lws = sbase + STGB;
#pragma unroll 1
    for (int s = 1; s < 10; ++s) {
      if (s >= 3) lds_wait(flg + 64 + (s - 3) * 4, 0u, 4u);
#pragma unroll
      for (int i = 0; i < 4; ++i) {
        glds16_saddr(xo0, xs + i * (16 * D_ * 2), lws + (unsigned)(STGW + (lw * 8 + 2 * i) * 1024));
        glds16_saddr(xo1, xs + i * (16 * D_ * 2) + 8 * D_ * 2, lws + (unsigned)(STGW + (lw * 8 + 2 * i + 1) * 1024));
      }
      const char* ptr = wsrc;
      unsigned dst = lws + (unsigned)(lw * 16 * 896);
      if (lane < 56) {
#pragma unroll
        for (int i = 0; i < 4; ++i) glds16_asm(ptr + (size_t)i * (2 * F_ * 2), dst + i * 896);
      }
      if (s >= 3) {
        asm volatile("s_waitcnt vmcnt(12)" ::: "memory");
        lds_signal(flg + (s - 1) * 4);
      }
      if (lane < 56) {
#pragma unroll
        for (int i = 4; i < 16; ++i) glds16_asm(ptr + (size_t)i * (2 * F_ * 2), dst + i * 896);
      }
      if (s == 1) BARRIER();
      if (s == 2) {
        asm volatile("s_waitcnt vmcnt(24)" ::: "memory");
        lds_signal(flg + 4);
      }
      wsrc += 64 * F_ * 2;
      xs += 128;
      lws = (lws == sbase + 2 * STGB) ? sbase : lws + STGB;
    }
    asm volatile("s_waitcnt vmcnt(0)" ::: "memory");
    lds_signal(flg + 9 * 4);
    STAMP(K1_DBG, 2);
    STAMP_RT(K1_DBG, 31);
    STAMP_OUT(K1_DBG, 0);
    return;
  }

  {
#pragma unroll
    for (int i = 0; i < 2; ++i) {
      glds16_saddr(xo0, xs + (w * 32 + i * 16) * (D_ * 2), sbase + (unsigned)(STGW + (w * 4 + 2 * i) * 1024));
      glds16_saddr(xo1, xs + (w * 32 + i * 16 + 8) * (D_ * 2), sbase + (unsigned)(STGW + (w * 4 + 2 * i + 1) * 1024));
    }
    const char* ptr = wsrc + (size_t)(w * 16) * F_ * 2;
    if (lane < 56) {
#pragma unroll
      for (int i = 0; i < 8; ++i) glds16_asm(ptr + (size_t)i * (2 * F_ * 2), sbase + (unsigned)((w * 8 + i) * 896));
    }
  }
  SBAR();
  BARRIER();
  SBAR();
  const int hi = lane >> 5, q = (lane & 15) >> 2, p = lane & 3;
  int tj[7];
  unsigned roff[7];
#pragma unroll
  for (int j = 0; j < 7; ++j) {
    int jj = j;
    if (w < 3) {
      if (j < 2) jj = 2 * w + j;
      else if ((j >> 1) == w) jj = j & 1;
    } else {
      if (j == 0) jj = 6;
      else if (j == 6) jj = 0;
    }
    tj[j] = jj;
    roff[j] = sbase + (unsigned)((8 * hi + q) * 448 + ((lane >> 4) & 1) * 224 + p * 8 + jj * 32);
  }
  unsigned aoff[4];
#pragma unroll
  for (int kq = 0; kq < 4; ++kq)
    aoff[kq] = sbase + (unsigned)(STGW + (w * 32 + (lane & 31)) * 128 + (((2 * kq + hi) ^ ((lane >> 1) & 7)) * 16));

  f32x16 acc[7];
#pragma unroll
  for (int c = 0; c < 7; ++c)
#pragma unroll
    for (int e = 0; e < 16; ++e) acc[c][e] = 0.f;
  f32x4 accs = {0.f, 0.f, 0.f, 0.f}, accc[2] = {{0.f, 0.f, 0.f, 0.f}, {0.f, 0.f, 0.f, 0.f}};
  const unsigned sv = ((lane & 15) == ((lane >> 4) & 1)) ? 0x3f803f80u : 0u;
  u32x4 sel = {sv, sv, sv, sv};
  asm volatile("" : "+v"(sel), "+v"(accs), "+v"(accc[0]), "+v"(accc[1]));
#pragma unroll
  for (int c = 0; c < 7; ++c) asm volatile("" : "+v"(acc[c]), "+v"(roff[c]));
#pragma unroll
  for (int c = 0; c < 4; ++c) asm volatile("" : "+v"(aoff[c]));
  unsigned fa = flg;
  STAMP(K1_DBG, 2);
  asm volatile("s_waitcnt vmcnt(0)" ::: "memory");
  lds_signal(fa);
  lds_wait(fa, 0u, 4u);
  STAMP(K1_DBG, 3);

  constexpr int RA = 6;
  u32x4 cw[RA], cx;
#pragma unroll
  for (int c = 0; c < RA; ++c) {
    u32x2 lo = tr_read_a(roff[c]);
    u32x2 hh = tr_read_a(roff[c] + 4 * 448);
    cw[c] = (u32x4){lo.x, lo.y, hh.x, hh.y};
    if (c == RA - 5) cx = lds_rd128(aoff[0]);
    SBAR();
  }
  int slot = 0;
#pragma unroll 1
  for (int s = 0; s < 10; ++s) {
    const unsigned dl = (slot == 2) ? (unsigned)(-2 * STGB) : (unsigned)STGB;
    slot = (slot == 2) ? 0 : slot + 1;
    const unsigned need = (s < 9) ? 2u : 0u;
    u32x4 xa[5], wf[28 + RA];
    unsigned fl = 0u;
#pragma unroll
    for (int c = 0; c < RA; ++c) wf[c] = cw[c];
    xa[0] = cx;
#pragma unroll
    for (int t = 0; t < 28; ++t) {
      const int kq = t / 7, c = t % 7;
      const int tn = t + RA, kn = (tn / 7) & 3, cn = tn % 7;
      if (tn == 28) lds_wait(fa + 4, fl, need);
      {
        u32x2 lo = tr_read_a(roff[cn] + kn * 16 * 448);
        u32x2 hh = tr_read_a(roff[cn] + kn * 16 * 448 + 4 * 448);
        wf[tn] = (u32x4){lo.x, lo.y, hh.x, hh.y};
        if (tn >= 21 && tn < 28) roff[cn] += dl;
      }
      if (tn == 27) lds_signal(fa + 64);
      if (tn == 24) fl = lds_flag(fa + 4);
      if (c == 2) {
        xa[kq + 1] = lds_rd128(aoff[(kq + 1) & 3]);
        if (kq < 3) aoff[kq + 1] += dl;
      }
      if (t == 0) aoff[0] += dl;
      SBAR();
      acc[c] = mfma32(wf[t], xa[kq], acc[c]);
      if (c < 2) accc[c] = mfma16(wf[t], sel, accc[c]);
      if (c == 2) accs = mfma16(sel, xa[kq], accs);
      SBAR();
    }
#pragma unroll
    for (int c = 0; c < RA; ++c) cw[c] = wf[28 + c];
    cx = xa[4];
    fa += 4;
    STAMP(K1_DBG, 4 + s);
  }

  STAMP(K1_DBG, 23);
  float* csb = (float*)(smem + K1_CS);
  if ((lane & 15) < 2) {
    float* cp = csb + (lane & 15) * 112 + 4 * (lane >> 4);
    *(f32x4*)(cp + tj[0] * 16) = accc[0];
    if (w < 3) *(f32x4*)(cp + tj[1] * 16) = accc[1];
  }
  lds_signal(flg + 128);
  float nmb;
  {
    float v0 = __shfl(accs[0], lane & 15), v1 = __shfl(accs[1], lane & 15);
    nmb = -bf16r(((lane & 16) ? v1 : v0) * (1.0f / D_));
  }
  lds_wait(flg + 128, 0u, 4u);
  char* hb = ws + WS_HT + ((size_t)((mt * 8 + w * 2 + ((lane >> 4) & 1)) * NKS2) * 64 + (lane & 15) + 16 * hi) * 16;
  const f32x2 nm2 = {nmb, nmb};
#pragma unroll
  for (int j = 0; j < 7; ++j) {
    const int jj = tj[j];
    const float* cp = csb + jj * 16 + 4 * hi;
    f32x4 c4[4] = {*(const f32x4*)(cp), *(const f32x4*)(cp + 8), *(const f32x4*)(cp + 112), *(const f32x4*)(cp + 120)};
    u32x4 o;
#pragma unroll
    for (int i = 0; i < 4; ++i) {
      f32x2 gv = {acc[j][2 * i], acc[j][2 * i + 1]}, uv = {acc[j][8 + 2 * i], acc[j][9 + 2 * i]};
      f32x2 cgv = {c4[i >> 1][(2 * i) & 3], c4[i >> 1][(2 * i + 1) & 3]};
      f32x2 cuv = {c4[2 + (i >> 1)][(2 * i) & 3], c4[2 + (i >> 1)][(2 * i + 1) & 3]};
      gv = __builtin_elementwise_fma(nm2, cgv, gv);
      uv = __builtin_elementwise_fma(nm2, cuv, uv);
      f32x2 hv = gv * uv;
      o[i] = pack_bf16(hv.x, hv.y);
    }
    int ks = 3 * ft + (jj >> 1), tb = jj & 1;
    if (jj == 6) { ks = 46 + (ft >> 1); tb = ft & 1; }
    if (ft == 15 && jj == 2) { ks = 53; tb = 1; }
    if (ft < 15 || jj < 3) *(u32x4*)(hb + (size_t)ks * 1024 + tb * 512) = o;
  }
  STAMP(K1_DBG, 24);
  STAMP_RT(K1_DBG, 31);
  STAMP_OUT(K1_DBG, 0);
}

constexpr int K2_LDS = 4 * 20 * 1024;
constexpr int NCONV = 160;

template <int OFF>
__device__ __forceinline__ void gload16s(u32x4& dst, unsigned voff, const void* sbase) {
  asm volatile("global_load_dwordx4 %0, %1, %2 offset:%3" : "=v"(dst) : "v"(voff), "s"(sbase), "i"(OFF) : "memory");
}
template <int OFF>
__device__ __forceinline__ void gload8s(u32x2& dst, unsigned voff, const void* sbase) {
  asm volatile("global_load_dwordx2 %0, %1, %2 offset:%3" : "=v"(dst) : "v"(voff), "s"(sbase), "i"(OFF) : "memory");
}
template <int N>
__device__ __forceinline__ float dpp_shr(float v) {
  return __builtin_bit_cast(float, __builtin_amdgcn_update_dpp(0, __builtin_bit_cast(int, v), 0x110 + N, 0xf, 0xf, true));
}
template <int N>
__device__ __forceinline__ float dpp_shl(float v) {
  return __builtin_bit_cast(float, __builtin_amdgcn_update_dpp(0, __builtin_bit_cast(int, v), 0x100 + N, 0xf, 0xf, true));
}

__global__ __launch_bounds__(256, 2) void k2_kernel(const u16* __restrict__ x, const u16* __restrict__ ssmk,
                                                    const u16* __restrict__ ssmd, char* ws, u16* __restrict__ out) {
  extern __shared__ __attribute__((aligned(16))) char smem[];
  const int w = __builtin_amdgcn_readfirstlane(threadIdx.x >> 6);
  const int lane = threadIdx.x & 63;
  const int g = lane >> 4;
  ST2_DECL;
  ST2_RT(0);
  ST2(1);

  if (blockIdx.x < NCONV) {
    const int cb = blockIdx.x;
    const int d = cb * 4 + w;
    char* lb = smem + w * 9216;
    char* Kd = lb + 256;
    char* Xd = lb + 5120;
#pragma unroll
    for (int i = 0; i < 4; ++i) {
      glds16(ssmk + (size_t)d * S_ + i * 512 + lane * 8, Kd + i * 1024);
      glds16(ws + WS_KVP + (size_t)d * 4096 + i * 1024 + lane * 16, Xd + i * 1024);
    }
    {
      u32x4 z = {0u, 0u, 0u, 0u};
      *(LAS u32x4*)(lb + ((lane < 16) ? lane * 16 : 4352 - 256 + lane * 16)) = z;
    }
    __builtin_amdgcn_s_setprio(3);
    const __bf16 k0 = (__bf16)(bf_lo((unsigned)ssmd[d]) + bf_lo((unsigned)ssmk[(size_t)d * S_]));
    asm volatile("s_waitcnt vmcnt(0) lgkmcnt(0)" ::: "memory");
    *(LAS u16*)Kd = __builtin_bit_cast(u16, k0);
    asm volatile("" ::: "memory");
    ST2(2);

    const int q = (lane & 15) >> 2, p = lane & 3;
    const unsigned kof = (unsigned)(256 - 64 * g - 8 * q + 8 * p);
    const int al = (lane >> 2) & 3, c = lane & 3;
    const unsigned xo0 = (unsigned)(5120 + 256 * al + 16 * kv_pos(g, al, c));
    const unsigned xo1 = (unsigned)(5120 + 256 * al + 16 * kv_pos(g, al + 1, c));

    f32x4 acc[8][4];
#pragma unroll
    for (int t = 0; t < 8; ++t)
#pragma unroll
      for (int G = 0; G < 4; ++G) acc[t][G] = (f32x4){0.f, 0.f, 0.f, 0.f};

#pragma unroll
    for (int ph = 0; ph < 4; ++ph) {
#pragma unroll 1
      for (int mh = 0; mh < 2; ++mh) {
#pragma unroll
        for (int mo = 0; mo < 2; ++mo) {
          const int mu = ph * 4 + mh * 2 + mo;
          u32x4 xf[4];
#pragma unroll
          for (int G = ph; G < 4; ++G)
            xf[G] = *(LAS u32x4*)(lb + (mo ? xo1 : xo0) + 256 * (4 * G - mu));
#pragma unroll
          for (int t = 0; t < 8; ++t) {
            u32x2 lo = tr_read(lb, kof + 256 * mu + 32 * t);
            u32x2 hi = tr_read(lb, kof + 256 * mu + 32 * t - 32);
            u32x4 kf = {lo.x, lo.y, hi.x, hi.y};
#pragma unroll
            for (int G = ph; G < 4; ++G) acc[t][G] = mfma16(xf[G], kf, acc[t][G]);
          }
        }
      }
    }
    ST2(3);
    const int i16 = lane & 15;
    char* ab = smem + 40960 + (128 * g + i16) * 8 + w * 2;
#pragma unroll
    for (int G = 0; G < 4; ++G) {
      float pv[4];
#pragma unroll
      for (int r = 1; r < 4; ++r) {
        float prov = (g == 3) ? ((G > 0) ? acc[7][G > 0 ? G - 1 : 0][r] : 0.f) : acc[7][G][r];
        pv[r] = __shfl(prov, (lane + 48) & 63);
      }
      float y[8];
#pragma unroll
      for (int t = 7; t >= 0; --t) {
        float p1 = (t > 0) ? acc[t > 0 ? t - 1 : 0][G][1] : pv[1];
        float p2 = (t > 0) ? acc[t > 0 ? t - 1 : 0][G][2] : pv[2];
        float p3 = (t > 0) ? acc[t > 0 ? t - 1 : 0][G][3] : pv[3];
        float v = acc[t][G][0] + dpp_shr<1>(acc[t][G][1]);
        v += dpp_shr<2>(acc[t][G][2]);
        v += dpp_shr<3>(acc[t][G][3]);
        v += dpp_shl<15>(p1);
        v += dpp_shl<14>(p2);
        v += dpp_shl<13>(p3);
        y[t] = v;
      }
#pragma unroll
      for (int t = 0; t < 8; t += 2) {
        unsigned pk = pack_bf16(y[t], y[t + 1]);
        *(LAS u16*)(ab + G * 4096 + t * 128) = (u16)pk;
        *(LAS u16*)(ab + G * 4096 + t * 128 + 128) = (u16)(pk >> 16);
      }
    }
    ST2(4);
    __syncthreads();
#pragma unroll
    for (int i = 0; i < 8; ++i) {
      int s = threadIdx.x + 256 * i;
      unsigned long long v = *(LAS unsigned long long*)(smem + 40960 + s * 8);
      if (v == ~0ull) v = ~1ull;
      size_t o = WS_YT + ((size_t)((s >> 4) * 40 + (cb >> 2)) * 64 + (s & 15) + 16 * (cb & 3)) * 8;
      __hip_atomic_store((GAS unsigned long long*)(ws + o), v, __ATOMIC_RELAXED, __HIP_MEMORY_SCOPE_AGENT);
    }
    ST2(5);
    ST2(6);
    ST2_RT(11);
    ST2_OUT();
    return;
  }

  const int b = blockIdx.x - NCONV;
  const int xcd = b & 7, bi = b >> 3;
  const int mt = (xcd >> 1) * 8 + (bi >> 2);
  const int nt = (xcd & 1) * 4 + (bi & 3);
  const int ksf = (w < 2) ? w * 14 : 27 + (w - 2) * 13;
  const char* hp = ws + WS_HT + (size_t)(mt * 4 * NKS2 + ksf) * 1024;
  const char* wp = ws + WS_WDT + (size_t)(nt * 5 * NKS2 + ksf) * 1024;
  const int m16 = mt * 4 + w;
  const int row = m16 * 16 + (lane & 15);

  u32x2 xv[5], stv;
  {
    const unsigned xo = (unsigned)((row * D_ + nt * 80 + 4 * g) * 2);
    gload8s<0>(xv[0], xo, x);
    gload8s<32>(xv[1], xo, x);
    gload8s<64>(xv[2], xo, x);
    gload8s<96>(xv[3], xo, x);
    gload8s<128>(xv[4], xo, x);
    gload8s<0>(stv, (unsigned)(row * 8), ws + WS_STATS);
  }

  f32x4 acc[4][5];
#pragma unroll
  for (int m = 0; m < 4; ++m)
#pragma unroll
    for (int n = 0; n < 5; ++n) acc[m][n] = (f32x4){0.f, 0.f, 0.f, 0.f};

  u32x4 hf[4][4], wf[4][5];
  unsigned vo[4];
#pragma unroll
  for (int i = 0; i < 4; ++i) vo[i] = (unsigned)(lane * 16 + i * 4096);
#define LOADK(j)                                                                           \
  do {                                                                                     \
    _Pragma("unroll") for (int m = 0; m < 4; ++m)                                          \
        gload16s<((j) & 3) * 1024>(hf[(j) & 3][m], vo[(j) >> 2], hp + (size_t)m * NKS2 * 1024); \
    _Pragma("unroll") for (int n = 0; n < 5; ++n)                                          \
        gload16s<((j) & 3) * 1024>(wf[(j) & 3][n], vo[(j) >> 2], wp + (size_t)n * NKS2 * 1024); \
  } while (0)
#define WAITK(j, N)                                                                        \
  asm volatile("s_waitcnt vmcnt(" #N ")"                                                   \
               : "+v"(hf[(j) & 3][0]), "+v"(hf[(j) & 3][1]), "+v"(hf[(j) & 3][2]), "+v"(hf[(j) & 3][3]), \
                 "+v"(wf[(j) & 3][0]), "+v"(wf[(j) & 3][1]), "+v"(wf[(j) & 3][2]), "+v"(wf[(j) & 3][3]), \
                 "+v"(wf[(j) & 3][4])::"memory")
#define MMAK(j)                                                                            \
  do {                                                                                     \
    _Pragma("unroll") for (int m = 0; m < 4; ++m)                                          \
        _Pragma("unroll") for (int n = 0; n < 5; ++n)                                      \
            acc[m][n] = mfma16(wf[(j) & 3][n], hf[(j) & 3][m], acc[m][n]);                 \
    SBAR();                                                                                \
  } while (0)
  if (w < 2) LOADK(0);
  LOADK(1);
  LOADK(2);
  LOADK(3);
  if (w < 2) { WAITK(0, 27); MMAK(0); }
  LOADK(4);  WAITK(1, 27);  MMAK(1);
  LOADK(5);  WAITK(2, 27);  MMAK(2);
  LOADK(6);  WAITK(3, 27);  MMAK(3);
  LOADK(7);  WAITK(4, 27);  MMAK(4);
  LOADK(8);  WAITK(5, 27);  MMAK(5);
  LOADK(9);  WAITK(6, 27);  MMAK(6);
  LOADK(10); WAITK(7, 27);  MMAK(7);
  LOADK(11); WAITK(8, 27);  MMAK(8);
  LOADK(12); WAITK(9, 27);  MMAK(9);
  LOADK(13); WAITK(10, 27); MMAK(10);
  WAITK(11, 18); MMAK(11);
  WAITK(12, 9);  MMAK(12);
  WAITK(13, 0);  MMAK(13);
  asm volatile("" : "+v"(xv[0]), "+v"(xv[1]), "+v"(xv[2]), "+v"(xv[3]), "+v"(xv[4]), "+v"(stv));
  ST2(3);
#pragma unroll
  for (int m = 0; m < 4; ++m)
#pragma unroll
    for (int n = 0; n < 5; ++n) *(LAS f32x4*)(smem + ((w * 20 + m * 5 + n) * 64 + lane) * 16) = acc[m][n];
  GAS unsigned long long* yp = (GAS unsigned long long*)(ws + WS_YT + ((size_t)(m16 * 40 + nt * 5) * 64 + lane) * 8);
  unsigned long long yv[5];
#pragma unroll
  for (int n = 0; n < 5; ++n) yv[n] = __hip_atomic_load(yp + n * 64, __ATOMIC_RELAXED, __HIP_MEMORY_SCOPE_AGENT);
  __builtin_amdgcn_s_waitcnt(0xC07F);
  __builtin_amdgcn_s_barrier();
  asm volatile("" ::: "memory");
  ST2(4);
  const float mean = __uint_as_float(stv.x), rden = 1.0f / __uint_as_float(stv.y);
  const f32x2 nm2 = {-mean, -mean}, rd2 = {rden, rden}, rs2 = {rden * rden, rden * rden};
  f32x2 ob[5][2], xn[5][2];
#pragma unroll
  for (int n = 0; n < 5; ++n) {
    f32x4 s = *(LAS f32x4*)(smem + ((0 * 20 + w * 5 + n) * 64 + lane) * 16);
#pragma unroll
    for (int ww = 1; ww < 4; ++ww) s += *(LAS f32x4*)(smem + ((ww * 20 + w * 5 + n) * 64 + lane) * 16);
    f32x2 x0 = {bf_lo(xv[n].x), bf_hi(xv[n].x)}, x1 = {bf_lo(xv[n].y), bf_hi(xv[n].y)};
    xn[n][0] = (x0 + nm2) * rd2;
    xn[n][1] = (x1 + nm2) * rd2;
    ob[n][0] = __builtin_elementwise_fma((f32x2){s[0], s[1]}, rs2, x0);
    ob[n][1] = __builtin_elementwise_fma((f32x2){s[2], s[3]}, rs2, x1);
  }
  while (true) {
    bool miss = false;
#pragma unroll
    for (int n = 0; n < 5; ++n) miss |= (yv[n] == ~0ull);
    if (__builtin_amdgcn_ballot_w64(miss) == 0) break;
    __builtin_amdgcn_s_sleep(1);
#pragma unroll
    for (int n = 0; n < 5; ++n) yv[n] = __hip_atomic_load(yp + n * 64, __ATOMIC_RELAXED, __HIP_MEMORY_SCOPE_AGENT);
  }
  ST2(5);
#pragma unroll
  for (int n = 0; n < 5; ++n) {
    unsigned y0 = (unsigned)yv[n], y1 = (unsigned)(yv[n] >> 32);
    f32x2 o0 = __builtin_elementwise_fma(xn[n][0], (f32x2){bf_lo(y0), bf_hi(y0)}, ob[n][0]);
    f32x2 o1 = __builtin_elementwise_fma(xn[n][1], (f32x2){bf_lo(y1), bf_hi(y1)}, ob[n][1]);
    u32x2 ov = {pack_bf16(o0.x, o0.y), pack_bf16(o1.x, o1.y)};
    *(u32x2*)(out + (size_t)row * D_ + (nt * 5 + n) * 16 + 4 * g) = ov;
  }
  ST2(6);
  ST2_RT(11);
  ST2_OUT();
}

extern "C" void kernel_launch(void* const* d_in, const int* in_sizes, int n_in, void* d_out, int out_size, void* d_ws,
                              size_t ws_size, hipStream_t stream) {
  const u16* q = (const u16*)d_in[0];
  const u16* k = (const u16*)d_in[1];
  const u16* v = (const u16*)d_in[2];
  const u16* shk = (const u16*)d_in[3];
  const u16* ssmk = (const u16*)d_in[4];
  const u16* ssmd = (const u16*)d_in[5];
  const u16* wg = (const u16*)d_in[7];
  const u16* wu = (const u16*)d_in[8];
  const u16* wd = (const u16*)d_in[9];
  (void)hipFuncSetAttribute((const void*)k1_kernel, hipFuncAttributeMaxDynamicSharedMemorySize, K1_LDS);
  (void)hipFuncSetAttribute((const void*)k2_kernel, hipFuncAttributeMaxDynamicSharedMemorySize, K2_LDS);
  k1_kernel<<<256, 512, K1_LDS, stream>>>(q, k, v, shk, wg, wu, wd, (char*)d_ws);
  k2_kernel<<<NCONV + 256, 256, K2_LDS, stream>>>(q, ssmk, ssmd, (char*)d_ws, (u16*)d_out);
}
```

```cpp
#include <hip/hip_runtime.h>
#include <hip/hip_bf16.h>
#include <stdint.h>

typedef __bf16 bf16x8 __attribute__((ext_vector_type(8)));
typedef __bf16 bf16x2 __attribute__((ext_vector_type(2)));
typedef short s16x4 __attribute__((ext_vector_type(4)));
typedef float f32x4 __attribute__((ext_vector_type(4)));
typedef float f32x16 __attribute__((ext_vector_type(16)));
typedef float f32x2 __attribute__((ext_vector_type(2)));
typedef unsigned u32x4 __attribute__((ext_vector_type(4)));
typedef unsigned u32x2 __attribute__((ext_vector_type(2)));
typedef unsigned short u16;
#define LAS __attribute__((address_space(3)))
#define GAS __attribute__((address_space(1)))

constexpr int S_ = 2048, D_ = 640, F_ = 1728;
constexpr int NKS2 = 54;

constexpr size_t WS_HT = 0;
constexpr size_t WS_WDT = WS_HT + (size_t)128 * NKS2 * 1024;
constexpr size_t WS_KVP = WS_WDT + (size_t)40 * NKS2 * 1024;
constexpr size_t WS_YT = WS_KVP + (size_t)D_ * 4096;
constexpr size_t WS_STATS = WS_YT + (size_t)S_ * D_ * 2;

__device__ __forceinline__ float bf16r(float x) { return (float)(__bf16)x; }
__device__ __forceinline__ unsigned pack_bf16(float lo, float hi) {
  f32x2 f = {lo, hi};
  return __builtin_bit_cast(unsigned, __builtin_convertvector(f, bf16x2));
}
__device__ __forceinline__ float bf_lo(unsigned u) { return __uint_as_float(u << 16); }
__device__ __forceinline__ float bf_hi(unsigned u) { return __uint_as_float(u & 0xffff0000u); }
__device__ __forceinline__ float dot2(unsigned a, unsigned b, float c) {
  return __builtin_amdgcn_fdot2_f32_bf16(__builtin_bit_cast(bf16x2, a), __builtin_bit_cast(bf16x2, b), c, false);
}
__device__ __forceinline__ f32x4 mfma16(u32x4 a, u32x4 b, f32x4 c) {
  return __builtin_amdgcn_mfma_f32_16x16x32_bf16(__builtin_bit_cast(bf16x8, a), __builtin_bit_cast(bf16x8, b), c, 0, 0, 0);
}
__device__ __forceinline__ u32x2 tr_read(const char* lds_base, unsigned off) {
  s16x4 v = __builtin_amdgcn_ds_read_tr16_b64_v4i16((LAS s16x4*)(lds_base + off));
  return __builtin_bit_cast(u32x2, v);
}
__device__ __forceinline__ void glds16(const void* g, char* l) {
  __builtin_amdgcn_global_load_lds((const GAS unsigned*)g, (LAS unsigned*)l, 16, 0, 0);
}
__device__ __forceinline__ void glds16_asm(const void* g, unsigned lds_dst) {
  unsigned keep;
  asm volatile("s_mov_b32 %0, m0\n\ts_mov_b32 m0, %2\n\ts_nop 0\n\tglobal_load_lds_dwordx4 %1, off\n\ts_mov_b32 m0, %0"
               : "=&s"(keep) : "v"(g), "s"(lds_dst) : "memory");
}
__device__ __forceinline__ u32x2 tr_read_a(unsigned addr) {
  s16x4 v = __builtin_amdgcn_ds_read_tr16_b64_v4i16((LAS s16x4*)addr);
  return __builtin_bit_cast(u32x2, v);
}
__device__ __forceinline__ unsigned kv_pos(unsigned g, unsigned alpha, unsigned c) {
  unsigned f = ((g & 1u) << 1) | ((alpha & 1u) ^ (g >> 1));
  return 4u * f + c;
}
__device__ __forceinline__ void kstep_tiles(int ks, int& fA, int& fB) {
  if (ks < 45) { int ft = ks / 3, p = ks - 3 * ft; fA = 112 * ft + 32 * p; fB = fA + 16; }
  else if (ks == 45) { fA = 1680; fB = 1696; }
  else { int e = ks - 46; fA = 224 * e + 96; fB = (e < 7) ? (224 * e + 208) : 1712; }
}

#define ST2_DECL
#define ST2(i)
#define ST2_RT(i)
#define ST2_OUT()
#define STAMP(a, b)
#define STAMP_RT(a, b)
#define STAMP_OUT(a, b)
#define DBG_LDS 0
constexpr int NSTG = 3;
constexpr int STGW = 64 * 448;
constexpr int STGB = STGW + 128 * 128;
constexpr int K1_SIDE = NSTG * STGB;
constexpr int K1_CS = K1_SIDE + 20480;
constexpr int K1_FLG = K1_CS + 1024;
constexpr int K1_DBG = K1_FLG + 256;
constexpr int K1_LDS = K1_DBG + DBG_LDS;

#define SBAR() __builtin_amdgcn_sched_barrier(0)
#define BARRIER() do { asm volatile("" ::: "memory"); __builtin_amdgcn_s_barrier(); asm volatile("" ::: "memory"); } while (0)

__device__ __forceinline__ void glds16_saddr(unsigned voff, const void* sbase, unsigned lds_dst) {
  unsigned keep;
  asm volatile("s_mov_b32 %0, m0\n\ts_mov_b32 m0, %3\n\ts_nop 0\n\tglobal_load_lds_dwordx4 %1, %2\n\ts_mov_b32 m0, %0"
               : "=&s"(keep) : "v"(voff), "s"(sbase), "s"(lds_dst) : "memory");
}
__device__ __forceinline__ void lds_signal(unsigned addr) {
  unsigned one = 1u;
  asm volatile("s_mov_b64 exec, 1\n\tds_add_u32 %0, %1\n\ts_mov_b64 exec, -1" ::"v"(addr), "v"(one) : "memory");
}
__device__ __forceinline__ unsigned lds_flag(unsigned addr) { return *(volatile LAS unsigned*)addr; }
#define WACC
#define WARG
__device__ __forceinline__ void lds_wait(unsigned addr, unsigned first, unsigned need WACC) {
  if (__builtin_expect((unsigned)__builtin_amdgcn_readfirstlane(first) < need, 0)) {
    unsigned f;
    do { f = lds_flag(addr); } while ((unsigned)__builtin_amdgcn_readfirstlane(f) < need);
  }
  asm volatile("" ::: "memory");
}
__device__ __forceinline__ u32x4 lds_rd128(unsigned addr) { return *(const LAS u32x4*)addr; }
__device__ __forceinline__ f32x16 mfma32(u32x4 a, u32x4 b, f32x16 c) {
  return __builtin_amdgcn_mfma_f32_32x32x16_bf16(__builtin_bit_cast(bf16x8, a), __builtin_bit_cast(bf16x8, b), c, 0, 0, 0);
}

__global__ __launch_bounds__(512, 2) void k1_kernel(const u16* __restrict__ x, const u16* __restrict__ kin,
                                                    const u16* __restrict__ vin, const u16* __restrict__ shk,
                                                    const u16* __restrict__ wg, const u16* __restrict__ wu,
                                                    const u16* __restrict__ wd, char* __restrict__ ws) {
  extern __shared__ __attribute__((aligned(16))) char smem[];
  const int b = blockIdx.x;
  const int w = __builtin_amdgcn_readfirstlane(threadIdx.x >> 6);
  const int lane = threadIdx.x & 63;
  const unsigned sbase = (unsigned)(size_t)(LAS char*)smem;
  const unsigned flg = sbase + K1_FLG;
  const int xcd = b & 7, bi = b >> 3;
  const int mt = (xcd >> 2) * 8 + (bi >> 2);
  const int ft = (xcd & 3) * 4 + (bi & 3);
  STAMP_RT(K1_DBG, 0);
  STAMP(K1_DBG, 1);
#define STAMPW(idx)
  if (w == 7) *(LAS unsigned*)(flg + lane * 4) = 0u;
  __builtin_amdgcn_s_waitcnt(0xC07F);
  SBAR();
  BARRIER();
  SBAR();

  if (w >= 6) {
    const int alpha = b >> 4, cg = b & 15;
    const int u = lane >> 5, m = lane & 31;
    const int s0 = alpha * 128 + 4 * m;
    while ((unsigned)__builtin_amdgcn_readfirstlane(lds_flag(flg)) < 4u) __builtin_amdgcn_s_sleep(4);
    asm volatile("" ::: "memory");
#pragma unroll 1
    for (int pass = 0; pass < 8 - w; ++pass) {
      const int cc = pass ? 4 : (w - 6) * 2 + u;
      const int d0 = cg * 40 + cc * 8;
      u32x4 kr[7], vr[4], sw[4];
#pragma unroll
      for (int i = 0; i < 7; ++i) {
        int s = s0 - 3 + i;
        u32x4 t = *(const u32x4*)(kin + (size_t)max(s, 0) * D_ + d0);
        u32x4 z = {0u, 0u, 0u, 0u};
        kr[i] = (s >= 0) ? t : z;
      }
#pragma unroll
      for (int i = 0; i < 4; ++i) vr[i] = *(const u32x4*)(vin + (size_t)(s0 + i) * D_ + d0);
#pragma unroll
      for (int i = 0; i < 4; ++i) sw[i] = *(const u32x4*)(shk + (size_t)d0 * 4 + i * 8);
      char* kb = smem + K1_SIDE + cc * 2048;
      const unsigned wb = (unsigned)((m & 7) * 2);
#pragma unroll
      for (int e = 0; e < 8; ++e) {
        unsigned t01 = sw[e >> 1][(e & 1) * 2], t23 = sw[e >> 1][(e & 1) * 2 + 1];
        float w0 = bf_lo(t01), w1 = bf_hi(t01), w2 = bf_lo(t23), w3 = bf_hi(t23);
        float kf[7];
#pragma unroll
        for (int i = 0; i < 7; ++i) { unsigned t = kr[i][e >> 1]; kf[i] = (e & 1) ? bf_hi(t) : bf_lo(t); }
#pragma unroll
        for (int c = 0; c < 4; ++c) {
          unsigned t = vr[c][e >> 1];
          float vv = (e & 1) ? bf_hi(t) : bf_lo(t);
          float a = kf[c] * w3;
          a = fmaf(kf[c + 1], w2, a);
          a = fmaf(kf[c + 2], w1, a);
          a = bf16r(fmaf(kf[c + 3], w0, a));
          __bf16 r = (__bf16)(a * vv);
          *(LAS u16*)(kb + e * 256 + kv_pos(m >> 3, alpha, c) * 16 + wb) = __builtin_bit_cast(u16, r);
        }
      }
      {
        const int ch0 = pass ? 32 : (w - 6) * 16;
#pragma unroll
        for (int i = 0; i < 4; ++i) {
          if (i < 2 || pass == 0) {
            int idx = i * 64 + lane;
            int chl = ch0 + (idx >> 4);
            u32x4 v = *(LAS u32x4*)(smem + K1_SIDE + chl * 256 + (idx & 15) * 16);
            *(u32x4*)(ws + WS_KVP + ((size_t)(cg * 40 + chl) * 16 + alpha) * 256 + (idx & 15) * 16) = v;
          }
        }
      }
    }
    if (w == 7) {
      u32x4 xr[10];
      {
        const u16* xp = x + (size_t)(b * 8 + (lane >> 3)) * D_ + (lane & 7) * 8;
#pragma unroll
        for (int i = 0; i < 10; ++i) xr[i] = *(const u32x4*)(xp + i * 64);
      }
      const bool dowd = b < 216;
      const int ks = b >> 2, nq = b & 3;
      char* lt = smem + K1_SIDE + 10240;
      if (dowd) {
        int fA, fB;
        kstep_tiles(ks, fA, fB);
#pragma unroll
        for (int i = 0; i < 10; ++i) {
          int idx = i * 64 + lane;
          int r = idx / 20, ch = idx - r * 20;
          int f = (r < 16) ? (fA + r) : (fB + r - 16);
          glds16_asm(wd + (size_t)f * D_ + nq * 160 + ch * 8, (unsigned)(size_t)(LAS char*)(lt + i * 1024));
        }
      }
      asm volatile("s_waitcnt vmcnt(0)" ::: "memory");
      if (dowd) {
        const int g = lane >> 4, q = (lane & 15) >> 2, p = lane & 3;
        const unsigned rb = (unsigned)((16 * (g >> 1) + 4 * (g & 1) + q) * 320 + p * 8);
#pragma unroll
        for (int n = 0; n < 10; ++n) {
          u32x2 lo = tr_read(lt, rb + n * 32);
          u32x2 hi = tr_read(lt, rb + n * 32 + 8 * 320);
          u32x4 v = {lo.x, lo.y, hi.x, hi.y};
          *(u32x4*)(ws + WS_WDT + ((size_t)((nq * 10 + n) * NKS2 + ks) * 64 + lane) * 16) = v;
        }
      }
      float sm = 0.f;
#pragma unroll
      for (int i = 0; i < 10; ++i)
#pragma unroll
        for (int e = 0; e < 4; ++e) sm += bf_lo(xr[i][e]) + bf_hi(xr[i][e]);
      sm += __shfl_xor(sm, 1);
      sm += __shfl_xor(sm, 2);
      sm += __shfl_xor(sm, 4);
      const float mean = sm * (1.0f / D_);
      float sq = 0.f;
#pragma unroll
      for (int i = 0; i < 10; ++i)
#pragma unroll
        for (int e = 0; e < 4; ++e) {
          float d0 = bf_lo(xr[i][e]) - mean, d1 = bf_hi(xr[i][e]) - mean;
          sq = fmaf(d0, d0, sq);
          sq = fmaf(d1, d1, sq);
        }
      sq += __shfl_xor(sq, 1);
      sq += __shfl_xor(sq, 2);
      sq += __shfl_xor(sq, 4);
      const float var = sq * (1.0f / D_);
      if ((lane & 7) == 0) {
        f32x2 st = {bf16r(mean), bf16r(sqrtf(bf16r(bf16r(var) + 1e-5f)))};
        *(f32x2*)(ws + WS_STATS + (size_t)(b * 8 + (lane >> 3)) * 8) = st;
      }
    } else {
      const u32x4 mk = {~0u, ~0u, ~0u, ~0u};
#pragma unroll
      for (int i = 0; i < 10; ++i) *(u32x4*)(ws + WS_YT + (size_t)b * 10240 + i * 1024 + lane * 16) = mk;
    }
    STAMP(K1_DBG, 2);
    STAMP_RT(K1_DBG, 31);
    STAMP_OUT(K1_DBG, 0);
    return;
  }

  const char* wsrc;
  {
    int r = lane >= 28, ch = lane - 28 * r;
    int mat = ch >= 14;
    int col = min(ft * 112 + (ch - 14 * mat) * 8, F_ - 8);
    wsrc = (const char*)((mat ? wu : wg) + (size_t)r * F_ + col);
  }
  const unsigned xo0 = (unsigned)((lane >> 3) * (D_ * 2) + (((lane & 7) ^ (lane >> 4)) * 16));
  const unsigned xo1 = xo0 ^ 64u;
  const char* xs = (const char*)(x + (size_t)(mt * 128) * D_);

  if (w >= 4) {
    const int lw = w - 4;
    __builtin_amdgcn_s_setprio(2);
    wsrc += (size_t)(64 + lw * 32) * F_ * 2;
    xs += lw * 64 * D_ * 2 + 128;
    unsigned lws = sbase + STGB;
    lds_wait(flg + 132, 0u, 4u WARG);
#pragma unroll 1
    for (int s = 1; s < 10; ++s) {
      if (s >= 3) lds_wait(flg + 64 + (s - 3) * 4, 0u, 4u WARG);
#pragma unroll
      for (int i = 0; i < 4; ++i) {
        glds16_saddr(xo0, xs + i * (16 * D_ * 2), lws + (unsigned)(STGW + (lw * 8 + 2 * i) * 1024));
        glds16_saddr(xo1, xs + i * (16 * D_ * 2) + 8 * D_ * 2, lws + (unsigned)(STGW + (lw * 8 + 2 * i + 1) * 1024));
      }
      const char* ptr = wsrc;
      unsigned dst = lws + (unsigned)(lw * 16 * 896);
      if (lane < 56) {
#pragma unroll
        for (int i = 0; i < 4; ++i) glds16_asm(ptr + (size_t)i * (2 * F_ * 2), dst + i * 896);
      }
      if (s >= 3) {
        asm volatile("s_waitcnt vmcnt(12)" ::: "memory");
        lds_signal(flg + (s - 1) * 4);
      }
      if (lane < 56) {
#pragma unroll
        for (int i = 4; i < 16; ++i) glds16_asm(ptr + (size_t)i * (2 * F_ * 2), dst + i * 896);
      }
      if (s == 2) {
        asm volatile("s_waitcnt vmcnt(24)" ::: "memory");
        lds_signal(flg + 4);
      }
      wsrc += 64 * F_ * 2;
      xs += 128;
      lws = (lws == sbase + 2 * STGB) ? sbase : lws + STGB;
    }
    asm volatile("s_waitcnt vmcnt(0)" ::: "memory");
    lds_signal(flg + 9 * 4);
    STAMPW(26);
    STAMP(K1_DBG, 2);
    STAMP_RT(K1_DBG, 31);
    STAMP_OUT(K1_DBG, 0);
    return;
  }

  __builtin_amdgcn_s_setprio(3);
  {
#pragma unroll
    for (int i = 0; i < 2; ++i) {
      glds16_saddr(xo0, xs + (w * 32 + i * 16) * (D_ * 2), sbase + (unsigned)(STGW + (w * 4 + 2 * i) * 1024));
      glds16_saddr(xo1, xs + (w * 32 + i * 16 + 8) * (D_ * 2), sbase + (unsigned)(STGW + (w * 4 + 2 * i + 1) * 1024));
    }
    const char* ptr = wsrc + (size_t)(w * 16) * F_ * 2;
    if (lane < 56) {
#pragma unroll
      for (int i = 0; i < 8; ++i) glds16_asm(ptr + (size_t)i * (2 * F_ * 2), sbase + (unsigned)((w * 8 + i) * 896));
    }
  }
  lds_signal(flg + 132);
  SBAR();
  const int hi = lane >> 5, q = (lane & 15) >> 2, p = lane & 3;
  int tj[7];
  unsigned roff[7];
#pragma unroll
  for (int j = 0; j < 7; ++j) {
    int jj = j;
    if (w < 3) {
      if (j < 2) jj = 2 * w + j;
      else if ((j >> 1) == w) jj = j & 1;
    } else {
      if (j == 0) jj = 6;
      else if (j == 6) jj = 0;
    }
    tj[j] = jj;
    roff[j] = sbase + (unsigned)((8 * hi + q) * 448 + ((lane >> 4) & 1) * 224 + p * 8 + jj * 32);
  }
  unsigned aoff[4];
#pragma unroll
  for (int kq = 0; kq < 4; ++kq)
    aoff[kq] = sbase + (unsigned)(STGW + (w * 32 + (lane & 31)) * 128 + (((2 * kq + hi) ^ ((lane >> 1) & 7)) * 16));

  f32x16 acc[7];
#pragma unroll
  for (int c = 0; c < 7; ++c)
#pragma unroll
    for (int e = 0; e < 16; ++e) acc[c][e] = 0.f;
  f32x4 accs = {0.f, 0.f, 0.f, 0.f}, accc[2] = {{0.f, 0.f, 0.f, 0.f}, {0.f, 0.f, 0.f, 0.f}};
  const unsigned sv = ((lane & 15) == ((lane >> 4) & 1)) ? 0x3f803f80u : 0u;
  u32x4 sel = {sv, sv, sv, sv};
  asm volatile("" : "+v"(sel), "+v"(accs), "+v"(accc[0]), "+v"(accc[1]));
#pragma unroll
  for (int c = 0; c < 7; ++c) asm volatile("" : "+v"(acc[c]), "+v"(roff[c]));
#pragma unroll
  for (int c = 0; c < 4; ++c) asm volatile("" : "+v"(aoff[c]));
  unsigned fa = flg;
  STAMP(K1_DBG, 2);
  asm volatile("s_waitcnt vmcnt(0)" ::: "memory");
  lds_signal(fa);
  lds_wait(fa, 0u, 4u WARG);
  STAMP(K1_DBG, 3);
  STAMPW(25);

  constexpr int RA = 6;
  u32x4 cw[RA], cx;
#pragma unroll
  for (int c = 0; c < RA; ++c) {
    u32x2 lo = tr_read_a(roff[c]);
    u32x2 hh = tr_read_a(roff[c] + 4 * 448);
    cw[c] = (u32x4){lo.x, lo.y, hh.x, hh.y};
    if (c == RA - 5) cx = lds_rd128(aoff[0]);
    SBAR();
  }
  int slot = 0;
#pragma unroll 1
  for (int s = 0; s < 10; ++s) {
    const unsigned dl = (slot == 2) ? (unsigned)(-2 * STGB) : (unsigned)STGB;
    slot = (slot == 2) ? 0 : slot + 1;
    const unsigned need = (s < 9) ? 2u : 0u;
    u32x4 xa[5], wf[28 + RA];
    unsigned fl = 0u;
#pragma unroll
    for (int c = 0; c < RA; ++c) wf[c] = cw[c];
    xa[0] = cx;
#pragma unroll
    for (int t = 0; t < 28; ++t) {
      const int kq = t / 7, c = t % 7;
      const int tn = t + RA, kn = (tn / 7) & 3, cn = tn % 7;
      if (tn == 28) lds_wait(fa + 4, fl, need WARG);
      {
        u32x2 lo = tr_read_a(roff[cn] + kn * 16 * 448);
        u32x2 hh = tr_read_a(roff[cn] + kn * 16 * 448 + 4 * 448);
        wf[tn] = (u32x4){lo.x, lo.y, hh.x, hh.y};
        if (tn >= 21 && tn < 28) roff[cn] += dl;
      }
      if (tn == 27) lds_signal(fa + 64);
      if (tn == 24) fl = lds_flag(fa + 4);
      if (c == 2) {
        xa[kq + 1] = lds_rd128(aoff[(kq + 1) & 3]);
        if (kq < 3) aoff[kq + 1] += dl;
      }
      if (t == 0) aoff[0] += dl;
      SBAR();
      acc[c] = mfma32(wf[t], xa[kq], acc[c]);
      if (c < 2) accc[c] = mfma16(wf[t], sel, accc[c]);
      if (c == 2) accs = mfma16(sel, xa[kq], accs);
      SBAR();
    }
#pragma unroll
    for (int c = 0; c < RA; ++c) cw[c] = wf[28 + c];
    cx = xa[4];
    fa += 4;
  }
  STAMPW(26);

  STAMP(K1_DBG, 23);
  float* csb = (float*)(smem + K1_CS);
  if ((lane & 15) < 2) {
    float* cp = csb + (lane & 15) * 112 + 4 * (lane >> 4);
    *(f32x4*)(cp + tj[0] * 16) = accc[0];
    if (w < 3) *(f32x4*)(cp + tj[1] * 16) = accc[1];
  }
  lds_signal(flg + 128);
  float nmb;
  {
    float v0 = __shfl(accs[0], lane & 15), v1 = __shfl(accs[1], lane & 15);
    nmb = -bf16r(((lane & 16) ? v1 : v0) * (1.0f / D_));
  }
  lds_wait(flg + 128, 0u, 4u WARG);
  char* hb = ws + WS_HT + ((size_t)((mt * 8 + w * 2 + ((lane >> 4) & 1)) * NKS2) * 64 + (lane & 15) + 16 * hi) * 16;
  const f32x2 nm2 = {nmb, nmb};
#pragma unroll
  for (int j = 0; j < 7; ++j) {
    const int jj = tj[j];
    const float* cp = csb + jj * 16 + 4 * hi;
    f32x4 c4[4] = {*(const f32x4*)(cp), *(const f32x4*)(cp + 8), *(const f32x4*)(cp + 112), *(const f32x4*)(cp + 120)};
    u32x4 o;
#pragma unroll
    for (int i = 0; i < 4; ++i) {
      f32x2 gv = {acc[j][2 * i], acc[j][2 * i + 1]}, uv = {acc[j][8 + 2 * i], acc[j][9 + 2 * i]};
      f32x2 cgv = {c4[i >> 1][(2 * i) & 3], c4[i >> 1][(2 * i + 1) & 3]};
      f32x2 cuv = {c4[2 + (i >> 1)][(2 * i) & 3], c4[2 + (i >> 1)][(2 * i + 1) & 3]};
      gv = __builtin_elementwise_fma(nm2, cgv, gv);
      uv = __builtin_elementwise_fma(nm2, cuv, uv);
      f32x2 hv = gv * uv;
      o[i] = pack_bf16(hv.x, hv.y);
    }
    int ks = 3 * ft + (jj >> 1), tb = jj & 1;
    if (jj == 6) { ks = 46 + (ft >> 1); tb = ft & 1; }
    if (ft == 15 && jj == 2) { ks = 53; tb = 1; }
    if (ft < 15 || jj < 3) *(u32x4*)(hb + (size_t)ks * 1024 + tb * 512) = o;
  }
  STAMP(K1_DBG, 24);
  STAMP_RT(K1_DBG, 31);
  STAMP_OUT(K1_DBG, 0);
}

constexpr int K2_LDS = 4 * 20 * 1024;
constexpr int NCONV = 160;

template <int OFF>
__device__ __forceinline__ void gload16s(u32x4& dst, unsigned voff, const void* sbase) {
  asm volatile("global_load_dwordx4 %0, %1, %2 offset:%3" : "=v"(dst) : "v"(voff), "s"(sbase), "i"(OFF) : "memory");
}
template <int OFF>
__device__ __forceinline__ void gload8s(u32x2& dst, unsigned voff, const void* sbase) {
  asm volatile("global_load_dwordx2 %0, %1, %2 offset:%3" : "=v"(dst) : "v"(voff), "s"(sbase), "i"(OFF) : "memory");
}
template <int N>
__device__ __forceinline__ float dpp_shr(float v) {
  return __builtin_bit_cast(float, __builtin_amdgcn_update_dpp(0, __builtin_bit_cast(int, v), 0x110 + N, 0xf, 0xf, true));
}
template <int N>
__device__ __forceinline__ float dpp_shl(float v) {
  return __builtin_bit_cast(float, __builtin_amdgcn_update_dpp(0, __builtin_bit_cast(int, v), 0x100 + N, 0xf, 0xf, true));
}

__global__ __launch_bounds__(256, 2) void k2_kernel(const u16* __restrict__ x, const u16* __restrict__ ssmk,
                                                    const u16* __restrict__ ssmd, char* ws, u16* __restrict__ out) {
  extern __shared__ __attribute__((aligned(16))) char smem[];
  const int w = __builtin_amdgcn_readfirstlane(threadIdx.x >> 6);
  const int lane = threadIdx.x & 63;
  const int g = lane >> 4;
  ST2_DECL;
  ST2_RT(0);
  ST2(1);

  if (blockIdx.x < NCONV) {
    const int cb = blockIdx.x;
    const int d = cb * 4 + w;
    char* lb = smem + w * 9216;
    char* Kd = lb + 256;
    char* Xd = lb + 5120;
#pragma unroll
    for (int i = 0; i < 4; ++i) {
      glds16(ssmk + (size_t)d * S_ + i * 512 + lane * 8, Kd + i * 1024);
      glds16(ws + WS_KVP + (size_t)d * 4096 + i * 1024 + lane * 16, Xd + i * 1024);
    }
    {
      u32x4 z = {0u, 0u, 0u, 0u};
      *(LAS u32x4*)(lb + ((lane < 16) ? lane * 16 : 4352 - 256 + lane * 16)) = z;
    }
    __builtin_amdgcn_s_setprio(3);
    const __bf16 k0 = (__bf16)(bf_lo((unsigned)ssmd[d]) + bf_lo((unsigned)ssmk[(size_t)d * S_]));

    const int q = (lane & 15) >> 2, p = lane & 3;
    const unsigned kof = (unsigned)(256 - 64 * g - 8 * q + 8 * p);
    const int al = (lane >> 2) & 3, c = lane & 3;
    const unsigned xo0 = (unsigned)(5120 + 256 * al + 16 * kv_pos(g, al, c));
    const unsigned xo1 = (unsigned)(5120 + 256 * al + 16 * kv_pos(g, al + 1, c));

    f32x4 acc[8][4];
#pragma unroll
    for (int t = 0; t < 8; ++t)
#pragma unroll
      for (int G = 0; G < 4; ++G) acc[t][G] = (f32x4){0.f, 0.f, 0.f, 0.f};
    const unsigned lbase = (unsigned)(size_t)(LAS char*)lb;
    unsigned ka = lbase + kof, kb = lbase + kof - 32, xa0 = lbase + xo0, xa1 = lbase + xo1;
    asm volatile("" : "+v"(ka), "+v"(kb), "+v"(xa0), "+v"(xa1));
#pragma unroll
    for (int t = 0; t < 8; ++t) asm volatile("" : "+v"(acc[t][0]), "+v"(acc[t][1]), "+v"(acc[t][2]), "+v"(acc[t][3]));
    asm volatile("s_waitcnt vmcnt(0) lgkmcnt(0)" ::: "memory");
    *(LAS u16*)Kd = __builtin_bit_cast(u16, k0);
    asm volatile("" ::: "memory");
    ST2(2);

    u32x4 xf[2][4], kf[2][4];
    auto ldx = [&](int mu) __attribute__((always_inline)) {
#pragma unroll
      for (int G = mu >> 2; G < 4; ++G) xf[mu & 1][G] = lds_rd128(((mu & 1) ? xa1 : xa0) + 256 * (4 * G - mu));
    };
    auto ldk = [&](int st) __attribute__((always_inline)) {
#pragma unroll
      for (int j = 0; j < 4; ++j) {
        u32x2 lo = tr_read_a(ka + 128 * st + 32 * j);
        u32x2 hi = tr_read_a(kb + 128 * st + 32 * j);
        kf[st & 1][j] = (u32x4){lo.x, lo.y, hi.x, hi.y};
      }
    };
    ldx(0);
    ldk(0);
#pragma unroll
    for (int st = 0; st < 32; ++st) {
      const int mu = st >> 1;
      if (st < 31) ldk(st + 1);
      if ((st & 1) == 0 && mu < 15) ldx(mu + 1);
      SBAR();
#pragma unroll
      for (int j = 0; j < 4; ++j)
#pragma unroll
        for (int G = mu >> 2; G < 4; ++G)
          acc[4 * (st & 1) + j][G] = mfma16(xf[mu & 1][G], kf[st & 1][j], acc[4 * (st & 1) + j][G]);
      SBAR();
    }
    asm volatile("s_nop 7\n\ts_nop 7\n\ts_nop 7" ::: "memory");
    ST2(3);
    const int i16 = lane & 15;
    char* ab = smem + 40960 + (128 * g + i16) * 8 + w * 2;
#pragma unroll
    for (int G = 0; G < 4; ++G) {
      float pv[4];
#pragma unroll
      for (int r = 1; r < 4; ++r) {
        float prov = (g == 3) ? ((G > 0) ? acc[7][G > 0 ? G - 1 : 0][r] : 0.f) : acc[7][G][r];
        pv[r] = __shfl(prov, (lane + 48) & 63);
      }
      float y[8];
#pragma unroll
      for (int t = 7; t >= 0; --t) {
        float p1 = (t > 0) ? acc[t > 0 ? t - 1 : 0][G][1] : pv[1];
        float p2 = (t > 0) ? acc[t > 0 ? t - 1 : 0][G][2] : pv[2];
        float p3 = (t > 0) ? acc[t > 0 ? t - 1 : 0][G][3] : pv[3];
        float v = acc[t][G][0];
        asm volatile(
            "s_nop 1\n\t"
            "v_add_f32_dpp %0, %1, %0 row_shr:1 row_mask:0xf bank_mask:0xf bound_ctrl:1\n\t"
            "v_add_f32_dpp %0, %2, %0 row_shr:2 row_mask:0xf bank_mask:0xf bound_ctrl:1\n\t"
            "v_add_f32_dpp %0, %3, %0 row_shr:3 row_mask:0xf bank_mask:0xf bound_ctrl:1\n\t"
            "v_add_f32_dpp %0, %4, %0 row_shl:15 row_mask:0xf bank_mask:0xf bound_ctrl:1\n\t"
            "v_add_f32_dpp %0, %5, %0 row_shl:14 row_mask:0xf bank_mask:0xf bound_ctrl:1\n\t"
            "v_add_f32_dpp %0, %6, %0 row_shl:13 row_mask:0xf bank_mask:0xf bound_ctrl:1"
            : "+v"(v)
            : "v"(acc[t][G][1]), "v"(acc[t][G][2]), "v"(acc[t][G][3]), "v"(p1), "v"(p2), "v"(p3));
        y[t] = v;
      }
#pragma unroll
      for (int t = 0; t < 8; t += 2) {
        unsigned pk = pack_bf16(y[t], y[t + 1]);
        *(LAS u16*)(ab + G * 4096 + t * 128) = (u16)pk;
        *(LAS u16*)(ab + G * 4096 + t * 128 + 128) = (u16)(pk >> 16);
      }
    }
    ST2(4);
    __syncthreads();
#pragma unroll
    for (int i = 0; i < 8; ++i) {
      int s = threadIdx.x + 256 * i;
      unsigned long long v = *(LAS unsigned long long*)(smem + 40960 + s * 8);
      if (v == ~0ull) v = ~1ull;
      size_t o = WS_YT + ((size_t)((s >> 4) * 40 + (cb >> 2)) * 64 + (s & 15) + 16 * (cb & 3)) * 8;
      __hip_atomic_store((GAS unsigned long long*)(ws + o), v, __ATOMIC_RELAXED, __HIP_MEMORY_SCOPE_AGENT);
    }
    ST2(5);
    ST2(6);
    ST2_RT(11);
    ST2_OUT();
    return;
  }

  const int b = blockIdx.x - NCONV;
  const int xcd = b & 7, bi = b >> 3;
  const int mt = (xcd >> 1) * 8 + (bi >> 2);
  const int nt = (xcd & 1) * 4 + (bi & 3);
  const int ksf = (w < 2) ? w * 14 : 27 + (w - 2) * 13;
  const char* hp = ws + WS_HT + (size_t)(mt * 4 * NKS2 + ksf) * 1024;
  const char* wp = ws + WS_WDT + (size_t)(nt * 5 * NKS2 + ksf) * 1024;
  const int m16 = mt * 4 + w;
  const int row = m16 * 16 + (lane & 15);

  u32x2 xv[5], stv;
  {
    const unsigned xo = (unsigned)((row * D_ + nt * 80 + 4 * g) * 2);
    gload8s<0>(xv[0], xo, x);
    gload8s<32>(xv[1], xo, x);
    gload8s<64>(xv[2], xo, x);
    gload8s<96>(xv[3], xo, x);
    gload8s<128>(xv[4], xo, x);
    gload8s<0>(stv, (unsigned)(row * 8), ws + WS_STATS);
  }

  f32x4 acc[4][5];
#pragma unroll
  for (int m = 0; m < 4; ++m)
#pragma unroll
    for (int n = 0; n < 5; ++n) acc[m][n] = (f32x4){0.f, 0.f, 0.f, 0.f};

  u32x4 hf[4][4], wf[4][5];
  unsigned vo[4];
#pragma unroll
  for (int i = 0; i < 4; ++i) vo[i] = (unsigned)(lane * 16 + i * 4096);
#define LOADK(j)                                                                           \
  do {                                                                                     \
    _Pragma("unroll") for (int m = 0; m < 4; ++m)                                          \
        gload16s<((j) & 3) * 1024>(hf[(j) & 3][m], vo[(j) >> 2], hp + (size_t)m * NKS2 * 1024); \
    _Pragma("unroll") for (int n = 0; n < 5; ++n)                                          \
        gload16s<((j) & 3) * 1024>(wf[(j) & 3][n], vo[(j) >> 2], wp + (size_t)n * NKS2 * 1024); \
  } while (0)
#define WAITK(j, N)                                                                        \
  asm volatile("s_waitcnt vmcnt(" #N ")"                                                   \
               : "+v"(hf[(j) & 3][0]), "+v"(hf[(j) & 3][1]), "+v"(hf[(j) & 3][2]), "+v"(hf[(j) & 3][3]), \
                 "+v"(wf[(j) & 3][0]), "+v"(wf[(j) & 3][1]), "+v"(wf[(j) & 3][2]), "+v"(wf[(j) & 3][3]), \
                 "+v"(wf[(j) & 3][4])::"memory")
#define MMAK(j)                                                                            \
  do {                                                                                     \
    _Pragma("unroll") for (int m = 0; m < 4; ++m)                                          \
        _Pragma("unroll") for (int n = 0; n < 5; ++n)                                      \
            acc[m][n] = mfma16(wf[(j) & 3][n], hf[(j) & 3][m], acc[m][n]);                 \
    SBAR();                                                                                \
  } while (0)
  if (w < 2) LOADK(0);
  LOADK(1);
  LOADK(2);
  LOADK(3);
  if (w < 2) { WAITK(0, 27); MMAK(0); }
  LOADK(4);  WAITK(1, 27);  MMAK(1);
  LOADK(5);  WAITK(2, 27);  MMAK(2);
  LOADK(6);  WAITK(3, 27);  MMAK(3);
  LOADK(7);  WAITK(4, 27);  MMAK(4);
  LOADK(8);  WAITK(5, 27);  MMAK(5);
  LOADK(9);  WAITK(6, 27);  MMAK(6);
  LOADK(10); WAITK(7, 27);  MMAK(7);
  LOADK(11); WAITK(8, 27);  MMAK(8);
  LOADK(12); WAITK(9, 27);  MMAK(9);
  LOADK(13); WAITK(10, 27); MMAK(10);
  WAITK(11, 18); MMAK(11);
  WAITK(12, 9);  MMAK(12);
  WAITK(13, 0);  MMAK(13);
  asm volatile("" : "+v"(xv[0]), "+v"(xv[1]), "+v"(xv[2]), "+v"(xv[3]), "+v"(xv[4]), "+v"(stv));
  ST2(3);
#pragma unroll
  for (int m = 0; m < 4; ++m)
#pragma unroll
    for (int n = 0; n < 5; ++n) *(LAS f32x4*)(smem + ((w * 20 + m * 5 + n) * 64 + lane) * 16) = acc[m][n];
  GAS unsigned long long* yp = (GAS unsigned long long*)(ws + WS_YT + ((size_t)(m16 * 40 + nt * 5) * 64 + lane) * 8);
  unsigned long long yv[5];
#pragma unroll
  for (int n = 0; n < 5; ++n) yv[n] = __hip_atomic_load(yp + n * 64, __ATOMIC_RELAXED, __HIP_MEMORY_SCOPE_AGENT);
  __builtin_amdgcn_s_waitcnt(0xC07F);
  __builtin_amdgcn_s_barrier();
  asm volatile("" ::: "memory");
  ST2(4);
  const float mean = __uint_as_float(stv.x), rden = 1.0f / __uint_as_float(stv.y);
  const f32x2 nm2 = {-mean, -mean}, rd2 = {rden, rden}, rs2 = {rden * rden, rden * rden};
  f32x2 ob[5][2], xn[5][2];
#pragma unroll
  for (int n = 0; n < 5; ++n) {
    f32x4 s = *(LAS f32x4*)(smem + ((0 * 20 + w * 5 + n) * 64 + lane) * 16);
#pragma unroll
    for (int ww = 1; ww < 4; ++ww) s += *(LAS f32x4*)(smem + ((ww * 20 + w * 5 + n) * 64 + lane) * 16);
    f32x2 x0 = {bf_lo(xv[n].x), bf_hi(xv[n].x)}, x1 = {bf_lo(xv[n].y), bf_hi(xv[n].y)};
    xn[n][0] = (x0 + nm2) * rd2;
    xn[n][1] = (x1 + nm2) * rd2;
    ob[n][0] = __builtin_elementwise_fma((f32x2){s[0], s[1]}, rs2, x0);
    ob[n][1] = __builtin_elementwise_fma((f32x2){s[2], s[3]}, rs2, x1);
  }
  while (true) {
    bool miss = false;
#pragma unroll
    for (int n = 0; n < 5; ++n) miss |= (yv[n] == ~0ull);
    if (__builtin_amdgcn_ballot_w64(miss) == 0) break;
    __builtin_amdgcn_s_sleep(1);
#pragma unroll
    for (int n = 0; n < 5; ++n) yv[n] = __hip_atomic_load(yp + n * 64, __ATOMIC_RELAXED, __HIP_MEMORY_SCOPE_AGENT);
  }
  ST2(5);
#pragma unroll
  for (int n = 0; n < 5; ++n) {
    unsigned y0 = (unsigned)yv[n], y1 = (unsigned)(yv[n] >> 32);
    f32x2 o0 = __builtin_elementwise_fma(xn[n][0], (f32x2){bf_lo(y0), bf_hi(y0)}, ob[n][0]);
    f32x2 o1 = __builtin_elementwise_fma(xn[n][1], (f32x2){bf_lo(y1), bf_hi(y1)}, ob[n][1]);
    u32x2 ov = {pack_bf16(o0.x, o0.y), pack_bf16(o1.x, o1.y)};
    *(u32x2*)(out + (size_t)row * D_ + (nt * 5 + n) * 16 + 4 * g) = ov;
  }
  ST2(6);
  ST2_RT(11);
  ST2_OUT();
}

extern "C" void kernel_launch(void* const* d_in, const int* in_sizes, int n_in, void* d_out, int out_size, void* d_ws,
                              size_t ws_size, hipStream_t stream) {
  const u16* q = (const u16*)d_in[0];
  const u16* k = (const u16*)d_in[1];
  const u16* v = (const u16*)d_in[2];
  const u16* shk = (const u16*)d_in[3];
  const u16* ssmk = (const u16*)d_in[4];
  const u16* ssmd = (const u16*)d_in[5];
  const u16* wg = (const u16*)d_in[7];
  const u16* wu = (const u16*)d_in[8];
  const u16* wd = (const u16*)d_in[9];
  (void)hipFuncSetAttribute((const void*)k1_kernel, hipFuncAttributeMaxDynamicSharedMemorySize, K1_LDS);
  (void)hipFuncSetAttribute((const void*)k2_kernel, hipFuncAttributeMaxDynamicSharedMemorySize, K2_LDS);
  k1_kernel<<<256, 512, K1_LDS, stream>>>(q, k, v, shk, wg, wu, wd, (char*)d_ws);
  k2_kernel<<<NCONV + 256, 256, K2_LDS, stream>>>(q, ssmk, ssmd, (char*)d_ws, (u16*)d_out);
}
```

```cpp
#include <hip/hip_runtime.h>
#include <hip/hip_bf16.h>
#include <stdint.h>

typedef __bf16 bf16x8 __attribute__((ext_vector_type(8)));
typedef __bf16 bf16x2 __attribute__((ext_vector_type(2)));
typedef short s16x4 __attribute__((ext_vector_type(4)));
typedef float f32x4 __attribute__((ext_vector_type(4)));
typedef float f32x16 __attribute__((ext_vector_type(16)));
typedef float f32x2 __attribute__((ext_vector_type(2)));
typedef unsigned u32x4 __attribute__((ext_vector_type(4)));
typedef unsigned u32x2 __attribute__((ext_vector_type(2)));
typedef unsigned short u16;
#define LAS __attribute__((address_space(3)))
#define GAS __attribute__((address_space(1)))

constexpr int S_ = 2048, D_ = 640, F_ = 1728;
constexpr int NKS2 = 54;

constexpr size_t WS_HT = 0;
constexpr size_t WS_WDT = WS_HT + (size_t)128 * NKS2 * 1024;
constexpr size_t WS_KVP = WS_WDT + (size_t)40 * NKS2 * 1024;
constexpr size_t WS_YT = WS_KVP + (size_t)D_ * 4096;
constexpr size_t WS_STATS = WS_YT + (size_t)S_ * D_ * 2;

__device__ __forceinline__ float bf16r(float x) { return (float)(__bf16)x; }
__device__ __forceinline__ unsigned pack_bf16(float lo, float hi) {
  f32x2 f = {lo, hi};
  return __builtin_bit_cast(unsigned, __builtin_convertvector(f, bf16x2));
}
__device__ __forceinline__ float bf_lo(unsigned u) { return __uint_as_float(u << 16); }
__device__ __forceinline__ float bf_hi(unsigned u) { return __uint_as_float(u & 0xffff0000u); }
__device__ __forceinline__ float dot2(unsigned a, unsigned b, float c) {
  return __builtin_amdgcn_fdot2_f32_bf16(__builtin_bit_cast(bf16x2, a), __builtin_bit_cast(bf16x2, b), c, false);
}
__device__ __forceinline__ f32x4 mfma16(u32x4 a, u32x4 b, f32x4 c) {
  return __builtin_amdgcn_mfma_f32_16x16x32_bf16(__builtin_bit_cast(bf16x8, a), __builtin_bit_cast(bf16x8, b), c, 0, 0, 0);
}
__device__ __forceinline__ u32x2 tr_read(const char* lds_base, unsigned off) {
  s16x4 v = __builtin_amdgcn_ds_read_tr16_b64_v4i16((LAS s16x4*)(lds_base + off));
  return __builtin_bit_cast(u32x2, v);
}
__device__ __forceinline__ void glds16(const void* g, char* l) {
  __builtin_amdgcn_global_load_lds((const GAS unsigned*)g, (LAS unsigned*)l, 16, 0, 0);
}
__device__ __forceinline__ void glds16_asm(const void* g, unsigned lds_dst) {
  unsigned keep;
  asm volatile("s_mov_b32 %0, m0\n\ts_mov_b32 m0, %2\n\ts_nop 0\n\tglobal_load_lds_dwordx4 %1, off\n\ts_mov_b32 m0, %0"
               : "=&s"(keep) : "v"(g), "s"(lds_dst) : "memory");
}
__device__ __forceinline__ u32x2 tr_read_a(unsigned addr) {
  s16x4 v = __builtin_amdgcn_ds_read_tr16_b64_v4i16((LAS s16x4*)addr);
  return __builtin_bit_cast(u32x2, v);
}
__device__ __forceinline__ unsigned kv_pos(unsigned g, unsigned alpha, unsigned c) {
  unsigned f = ((g & 1u) << 1) | ((alpha & 1u) ^ (g >> 1));
  return 4u * f + c;
}
__device__ __forceinline__ void kstep_tiles(int ks, int& fA, int& fB) {
  if (ks < 45) { int ft = ks / 3, p = ks - 3 * ft; fA = 112 * ft + 32 * p; fB = fA + 16; }
  else if (ks == 45) { fA = 1680; fB = 1696; }
  else { int e = ks - 46; fA = 224 * e + 96; fB = (e < 7) ? (224 * e + 208) : 1712; }
}

#define ST2_DECL
#define ST2(i)
#define ST2_RT(i)
#define ST2_OUT()
#define STAMP(a, b)
#define STAMP_RT(a, b)
#define STAMP_OUT(a, b)
#define DBG_LDS 0
constexpr int NSTG = 4;
constexpr int STGB = 64 * 448;
constexpr int K1_SIDE = NSTG * STGB;
constexpr int K1_CS = K1_SIDE + 20480;
constexpr int K1_FLG = K1_CS + 1024;
constexpr int K1_DBG = K1_FLG + 256;
constexpr int K1_LDS = K1_DBG + DBG_LDS;

#define SBAR() __builtin_amdgcn_sched_barrier(0)
#define BARRIER() do { asm volatile("" ::: "memory"); __builtin_amdgcn_s_barrier(); asm volatile("" ::: "memory"); } while (0)

__device__ __forceinline__ void glds16_saddr(unsigned voff, const void* sbase, unsigned lds_dst) {
  unsigned keep;
  asm volatile("s_mov_b32 %0, m0\n\ts_mov_b32 m0, %3\n\ts_nop 0\n\tglobal_load_lds_dwordx4 %1, %2\n\ts_mov_b32 m0, %0"
               : "=&s"(keep) : "v"(voff), "s"(sbase), "s"(lds_dst) : "memory");
}
__device__ __forceinline__ void lds_signal(unsigned addr) {
  unsigned one = 1u;
  asm volatile("s_mov_b64 exec, 1\n\tds_add_u32 %0, %1\n\ts_mov_b64 exec, -1" ::"v"(addr), "v"(one) : "memory");
}
__device__ __forceinline__ unsigned lds_flag(unsigned addr) { return *(volatile LAS unsigned*)addr; }
#define WACC
#define WARG
__device__ __forceinline__ void lds_wait(unsigned addr, unsigned first, unsigned need WACC) {
  if (__builtin_expect((unsigned)__builtin_amdgcn_readfirstlane(first) < need, 0)) {
    unsigned f;
    do { f = lds_flag(addr); } while ((unsigned)__builtin_amdgcn_readfirstlane(f) < need);
  }
  asm volatile("" ::: "memory");
}
__device__ __forceinline__ u32x4 lds_rd128(unsigned addr) { return *(const LAS u32x4*)addr; }
__device__ __forceinline__ f32x16 mfma32(u32x4 a, u32x4 b, f32x16 c) {
  return __builtin_amdgcn_mfma_f32_32x32x16_bf16(__builtin_bit_cast(bf16x8, a), __builtin_bit_cast(bf16x8, b), c, 0, 0, 0);
}

__global__ __launch_bounds__(512, 2) void k1_kernel(const u16* __restrict__ x, const u16* __restrict__ kin,
                                                    const u16* __restrict__ vin, const u16* __restrict__ shk,
                                                    const u16* __restrict__ wg, const u16* __restrict__ wu,
                                                    const u16* __restrict__ wd, char* __restrict__ ws) {
  extern __shared__ __attribute__((aligned(16))) char smem[];
  const int b = blockIdx.x;
  const int w = __builtin_amdgcn_readfirstlane(threadIdx.x >> 6);
  const int lane = threadIdx.x & 63;
  const unsigned sbase = (unsigned)(size_t)(LAS char*)smem;
  const unsigned flg = sbase + K1_FLG;
  const int xcd = b & 7, bi = b >> 3;
  const int mt = (xcd >> 2) * 8 + (bi >> 2);
  const int ft = (xcd & 3) * 4 + (bi & 3);
  STAMP_RT(K1_DBG, 0);
  STAMP(K1_DBG, 1);
#define STAMPW(idx)
  if (w == 7) *(LAS unsigned*)(flg + lane * 4) = 0u;
  __builtin_amdgcn_s_waitcnt(0xC07F);
  SBAR();
  BARRIER();
  SBAR();

  if (w >= 6) {
    const int alpha = b >> 4, cg = b & 15;
    const int u = lane >> 5, m = lane & 31;
    const int s0 = alpha * 128 + 4 * m;
    while ((unsigned)__builtin_amdgcn_readfirstlane(lds_flag(flg)) < 4u) __builtin_amdgcn_s_sleep(4);
    asm volatile("" ::: "memory");
#pragma unroll 1
    for (int pass = 0; pass < 8 - w; ++pass) {
      const int cc = pass ? 4 : (w - 6) * 2 + u;
      const int d0 = cg * 40 + cc * 8;
      u32x4 kr[7], vr[4], sw[4];
#pragma unroll
      for (int i = 0; i < 7; ++i) {
        int s = s0 - 3 + i;
        u32x4 t = *(const u32x4*)(kin + (size_t)max(s, 0) * D_ + d0);
        u32x4 z = {0u, 0u, 0u, 0u};
        kr[i] = (s >= 0) ? t : z;
      }
#pragma unroll
      for (int i = 0; i < 4; ++i) vr[i] = *(const u32x4*)(vin + (size_t)(s0 + i) * D_ + d0);
#pragma unroll
      for (int i = 0; i < 4; ++i) sw[i] = *(const u32x4*)(shk + (size_t)d0 * 4 + i * 8);
      char* kb = smem + K1_SIDE + cc * 2048;
      const unsigned wb = (unsigned)((m & 7) * 2);
#pragma unroll
      for (int e = 0; e < 8; ++e) {
        unsigned t01 = sw[e >> 1][(e & 1) * 2], t23 = sw[e >> 1][(e & 1) * 2 + 1];
        float w0 = bf_lo(t01), w1 = bf_hi(t01), w2 = bf_lo(t23), w3 = bf_hi(t23);
        float kf[7];
#pragma unroll
        for (int i = 0; i < 7; ++i) { unsigned t = kr[i][e >> 1]; kf[i] = (e & 1) ? bf_hi(t) : bf_lo(t); }
#pragma unroll
        for (int c = 0; c < 4; ++c) {
          unsigned t = vr[c][e >> 1];
          float vv = (e & 1) ? bf_hi(t) : bf_lo(t);
          float a = kf[c] * w3;
          a = fmaf(kf[c + 1], w2, a);
          a = fmaf(kf[c + 2], w1, a);
          a = bf16r(fmaf(kf[c + 3], w0, a));
          __bf16 r = (__bf16)(a * vv);
          *(LAS u16*)(kb + e * 256 + kv_pos(m >> 3, alpha, c) * 16 + wb) = __builtin_bit_cast(u16, r);
        }
      }
      {
        const int ch0 = pass ? 32 : (w - 6) * 16;
#pragma unroll
        for (int i = 0; i < 4; ++i) {
          if (i < 2 || pass == 0) {
            int idx = i * 64 + lane;
            int chl = ch0 + (idx >> 4);
            u32x4 v = *(LAS u32x4*)(smem + K1_SIDE + chl * 256 + (idx & 15) * 16);
            *(u32x4*)(ws + WS_KVP + ((size_t)(cg * 40 + chl) * 16 + alpha) * 256 + (idx & 15) * 16) = v;
          }
        }
      }
    }
    if (w == 7) {
      u32x4 xr[10];
      {
        const u16* xp = x + (size_t)(b * 8 + (lane >> 3)) * D_ + (lane & 7) * 8;
#pragma unroll
        for (int i = 0; i < 10; ++i) xr[i] = *(const u32x4*)(xp + i * 64);
      }
      const bool dowd = b < 216;
      const int ks = b >> 2, nq = b & 3;
      char* lt = smem + K1_SIDE + 10240;
      if (dowd) {
        int fA, fB;
        kstep_tiles(ks, fA, fB);
#pragma unroll
        for (int i = 0; i < 10; ++i) {
          int idx = i * 64 + lane;
          int r = idx / 20, ch = idx - r * 20;
          int f = (r < 16) ? (fA + r) : (fB + r - 16);
          glds16_asm(wd + (size_t)f * D_ + nq * 160 + ch * 8, (unsigned)(size_t)(LAS char*)(lt + i * 1024));
        }
      }
      asm volatile("s_waitcnt vmcnt(0)" ::: "memory");
      if (dowd) {
        const int g = lane >> 4, q = (lane & 15) >> 2, p = lane & 3;
        const unsigned rb = (unsigned)((16 * (g >> 1) + 4 * (g & 1) + q) * 320 + p * 8);
#pragma unroll
        for (int n = 0; n < 10; ++n) {
          u32x2 lo = tr_read(lt, rb + n * 32);
          u32x2 hi = tr_read(lt, rb + n * 32 + 8 * 320);
          u32x4 v = {lo.x, lo.y, hi.x, hi.y};
          *(u32x4*)(ws + WS_WDT + ((size_t)((nq * 10 + n) * NKS2 + ks) * 64 + lane) * 16) = v;
        }
      }
      float sm = 0.f;
#pragma unroll
      for (int i = 0; i < 10; ++i)
#pragma unroll
        for (int e = 0; e < 4; ++e) sm += bf_lo(xr[i][e]) + bf_hi(xr[i][e]);
      sm += __shfl_xor(sm, 1);
      sm += __shfl_xor(sm, 2);
      sm += __shfl_xor(sm, 4);
      const float mean = sm * (1.0f / D_);
      float sq = 0.f;
#pragma unroll
      for (int i = 0; i < 10; ++i)
#pragma unroll
        for (int e = 0; e < 4; ++e) {
          float d0 = bf_lo(xr[i][e]) - mean, d1 = bf_hi(xr[i][e]) - mean;
          sq = fmaf(d0, d0, sq);
          sq = fmaf(d1, d1, sq);
        }
      sq += __shfl_xor(sq, 1);
      sq += __shfl_xor(sq, 2);
      sq += __shfl_xor(sq, 4);
      const float var = sq * (1.0f / D_);
      if ((lane & 7) == 0) {
        f32x2 st = {bf16r(mean), bf16r(sqrtf(bf16r(bf16r(var) + 1e-5f)))};
        *(f32x2*)(ws + WS_STATS + (size_t)(b * 8 + (lane >> 3)) * 8) = st;
      }
    } else {
      const u32x4 mk = {~0u, ~0u, ~0u, ~0u};
#pragma unroll
      for (int i = 0; i < 10; ++i) *(u32x4*)(ws + WS_YT + (size_t)b * 10240 + i * 1024 + lane * 16) = mk;
    }
    STAMP(K1_DBG, 2);
    STAMP_RT(K1_DBG, 31);
    STAMP_OUT(K1_DBG, 0);
    return;
  }

  const char* wsrc;
  {
    int r = lane >= 28, ch = lane - 28 * r;
    int mat = ch >= 14;
    int col = min(ft * 112 + (ch - 14 * mat) * 8, F_ - 8);
    wsrc = (const char*)((mat ? wu : wg) + (size_t)r * F_ + col);
  }

  if (w >= 4) {
    const int lw = w - 4;
    __builtin_amdgcn_s_setprio(2);
    wsrc += (size_t)(64 + lw * 32) * F_ * 2;
    unsigned dst = sbase + STGB + (unsigned)(lw * 16 * 896);
    lds_wait(flg + 132, 0u, 4u WARG);
#pragma unroll 1
    for (int s = 1; s < 10; ++s) {
      if (s >= NSTG) lds_wait(flg + 64 + (s - NSTG) * 4, 0u, 4u WARG);
      if (lane < 56) {
#pragma unroll
        for (int i = 0; i < 8; ++i) glds16_asm(wsrc + (size_t)i * (2 * F_ * 2), dst + i * 896);
      }
      if (s >= 3) {
        asm volatile("s_waitcnt vmcnt(24)" ::: "memory");
        lds_signal(flg + (s - 2) * 4);
      }
      if (lane < 56) {
#pragma unroll
        for (int i = 8; i < 16; ++i) glds16_asm(wsrc + (size_t)i * (2 * F_ * 2), dst + i * 896);
      }
      wsrc += 64 * F_ * 2;
      dst = ((s & 3) == 3) ? dst - 3 * STGB : dst + STGB;
    }
    asm volatile("s_waitcnt vmcnt(16)" ::: "memory");
    lds_signal(flg + 8 * 4);
    asm volatile("s_waitcnt vmcnt(0)" ::: "memory");
    lds_signal(flg + 9 * 4);
    STAMPW(26);
    STAMP(K1_DBG, 2);
    STAMP_RT(K1_DBG, 31);
    STAMP_OUT(K1_DBG, 0);
    return;
  }

  __builtin_amdgcn_s_setprio(3);
  const int hi = lane >> 5, q = (lane & 15) >> 2, p = lane & 3;
  {
    const char* ptr = wsrc + (size_t)(w * 16) * F_ * 2;
    if (lane < 56) {
#pragma unroll
      for (int i = 0; i < 8; ++i) glds16_asm(ptr + (size_t)i * (2 * F_ * 2), sbase + (unsigned)((w * 8 + i) * 896));
    }
  }
  lds_signal(flg + 132);
  const u16* xp = x + (size_t)(mt * 128 + w * 32 + (lane & 31)) * D_ + hi * 32;
  u32x4 xa[4];
#pragma unroll
  for (int kq = 0; kq < 4; ++kq) xa[kq] = *(const u32x4*)(xp + kq * 8);
  SBAR();
  int tj[7];
  unsigned roff[7];
#pragma unroll
  for (int j = 0; j < 7; ++j) {
    int jj = j;
    if (w < 3) {
      if (j < 2) jj = 2 * w + j;
      else if ((j >> 1) == w) jj = j & 1;
    } else {
      if (j == 0) jj = 6;
      else if (j == 6) jj = 0;
    }
    tj[j] = jj;
    roff[j] = sbase + (unsigned)((32 * hi + q) * 448 + ((lane >> 4) & 1) * 224 + p * 8 + jj * 32);
  }

  f32x16 acc[7];
#pragma unroll
  for (int c = 0; c < 7; ++c)
#pragma unroll
    for (int e = 0; e < 16; ++e) acc[c][e] = 0.f;
  f32x4 accs = {0.f, 0.f, 0.f, 0.f}, accc[2] = {{0.f, 0.f, 0.f, 0.f}, {0.f, 0.f, 0.f, 0.f}};
  const unsigned sv = ((lane & 15) == ((lane >> 4) & 1)) ? 0x3f803f80u : 0u;
  u32x4 sel = {sv, sv, sv, sv};
  asm volatile("" : "+v"(sel), "+v"(accs), "+v"(accc[0]), "+v"(accc[1]));
#pragma unroll
  for (int c = 0; c < 7; ++c) asm volatile("" : "+v"(acc[c]), "+v"(roff[c]));
  unsigned fa = flg;
  STAMP(K1_DBG, 2);
  asm volatile("s_waitcnt vmcnt(4)" ::: "memory");
  lds_signal(fa);
  lds_wait(fa, 0u, 4u WARG);
  STAMP(K1_DBG, 3);
  STAMPW(25);

  constexpr int RA = 6;
  u32x4 cw[RA];
#pragma unroll
  for (int c = 0; c < RA; ++c) {
    u32x2 lo = tr_read_a(roff[c]);
    u32x2 hh = tr_read_a(roff[c] + 4 * 448);
    cw[c] = (u32x4){lo.x, lo.y, hh.x, hh.y};
    SBAR();
  }
#pragma unroll 1
  for (int s = 0; s < 10; ++s) {
    const unsigned dl = ((s & 3) == 3) ? (unsigned)(-3 * STGB) : (unsigned)STGB;
    const unsigned need = (s < 9) ? 2u : 0u;
    xp += 64;
    u32x4 wf[28 + RA];
    unsigned fl = 0u;
#pragma unroll
    for (int c = 0; c < RA; ++c) wf[c] = cw[c];
#pragma unroll
    for (int t = 0; t < 28; ++t) {
      const int kq = t / 7, c = t % 7;
      const int tn = t + RA, kn = (tn / 7) & 3, cn = tn % 7;
      if (tn == 28) lds_wait(fa + 4, fl, need WARG);
      {
        u32x2 lo = tr_read_a(roff[cn] + kn * 8 * 448);
        u32x2 hh = tr_read_a(roff[cn] + kn * 8 * 448 + 4 * 448);
        wf[tn] = (u32x4){lo.x, lo.y, hh.x, hh.y};
        if (tn >= 21 && tn < 28) roff[cn] += dl;
      }
      if (tn == 27) lds_signal(fa + 64);
      if (tn == 24) fl = lds_flag(fa + 4);
      SBAR();
      acc[c] = mfma32(wf[t], xa[kq], acc[c]);
      if (c < 2) accc[c] = mfma16(wf[t], sel, accc[c]);
      if (c == 2) accs = mfma16(sel, xa[kq], accs);
      SBAR();
      if (c == 6) {
        if (s < 9) xa[kq] = *(const u32x4*)(xp + kq * 8);
        SBAR();
      }
    }
#pragma unroll
    for (int c = 0; c < RA; ++c) cw[c] = wf[28 + c];
    fa += 4;
  }
  STAMPW(26);

  STAMP(K1_DBG, 23);
  float* csb = (float*)(smem + K1_CS);
  if ((lane & 15) < 2) {
    float* cp = csb + (lane & 15) * 112 + 4 * (lane >> 4);
    *(f32x4*)(cp + tj[0] * 16) = accc[0];
    if (w < 3) *(f32x4*)(cp + tj[1] * 16) = accc[1];
  }
  lds_signal(flg + 128);
  float nmb;
  {
    float v0 = __shfl(accs[0], lane & 15), v1 = __shfl(accs[1], lane & 15);
    nmb = -bf16r(((lane & 16) ? v1 : v0) * (1.0f / D_));
  }
  lds_wait(flg + 128, 0u, 4u WARG);
  char* hb = ws + WS_HT + ((size_t)((mt * 8 + w * 2 + ((lane >> 4) & 1)) * NKS2) * 64 + (lane & 15) + 16 * hi) * 16;
  const f32x2 nm2 = {nmb, nmb};
#pragma unroll
  for (int j = 0; j < 7; ++j) {
    const int jj = tj[j];
    const float* cp = csb + jj * 16 + 4 * hi;
    f32x4 c4[4] = {*(const f32x4*)(cp), *(const f32x4*)(cp + 8), *(const f32x4*)(cp + 112), *(const f32x4*)(cp + 120)};
    u32x4 o;
#pragma unroll
    for (int i = 0; i < 4; ++i) {
      f32x2 gv = {acc[j][2 * i], acc[j][2 * i + 1]}, uv = {acc[j][8 + 2 * i], acc[j][9 + 2 * i]};
      f32x2 cgv = {c4[i >> 1][(2 * i) & 3], c4[i >> 1][(2 * i + 1) & 3]};
      f32x2 cuv = {c4[2 + (i >> 1)][(2 * i) & 3], c4[2 + (i >> 1)][(2 * i + 1) & 3]};
      gv = __builtin_elementwise_fma(nm2, cgv, gv);
      uv = __builtin_elementwise_fma(nm2, cuv, uv);
      f32x2 hv = gv * uv;
      o[i] = pack_bf16(hv.x, hv.y);
    }
    int ks = 3 * ft + (jj >> 1), tb = jj & 1;
    if (jj == 6) { ks = 46 + (ft >> 1); tb = ft & 1; }
    if (ft == 15 && jj == 2) { ks = 53; tb = 1; }
    if (ft < 15 || jj < 3) *(u32x4*)(hb + (size_t)ks * 1024 + tb * 512) = o;
  }
  STAMP(K1_DBG, 24);
  STAMP_RT(K1_DBG, 31);
  STAMP_OUT(K1_DBG, 0);
}

constexpr int K2_LDS = 4 * 20 * 1024;
constexpr int NCONV = 160;

template <int OFF>
__device__ __forceinline__ void gload16s(u32x4& dst, unsigned voff, const void* sbase) {
  asm volatile("global_load_dwordx4 %0, %1, %2 offset:%3" : "=v"(dst) : "v"(voff), "s"(sbase), "i"(OFF) : "memory");
}
template <int OFF>
__device__ __forceinline__ void gload8s(u32x2& dst, unsigned voff, const void* sbase) {
  asm volatile("global_load_dwordx2 %0, %1, %2 offset:%3" : "=v"(dst) : "v"(voff), "s"(sbase), "i"(OFF) : "memory");
}
template <int N>
__device__ __forceinline__ float dpp_shr(float v) {
  return __builtin_bit_cast(float, __builtin_amdgcn_update_dpp(0, __builtin_bit_cast(int, v), 0x110 + N, 0xf, 0xf, true));
}
template <int N>
__device__ __forceinline__ float dpp_shl(float v) {
  return __builtin_bit_cast(float, __builtin_amdgcn_update_dpp(0, __builtin_bit_cast(int, v), 0x100 + N, 0xf, 0xf, true));
}

__global__ __launch_bounds__(256, 2) void k2_kernel(const u16* __restrict__ x, const u16* __restrict__ ssmk,
                                                    const u16* __restrict__ ssmd, char* ws, u16* __restrict__ out) {
  extern __shared__ __attribute__((aligned(16))) char smem[];
  const int w = __builtin_amdgcn_readfirstlane(threadIdx.x >> 6);
  const int lane = threadIdx.x & 63;
  const int g = lane >> 4;
  ST2_DECL;
  ST2_RT(0);
  ST2(1);

  if (blockIdx.x < NCONV) {
    const int cb = blockIdx.x;
    const int d = cb * 4 + w;
    char* lb = smem + w * 9216;
    char* Kd = lb + 256;
    char* Xd = lb + 5120;
#pragma unroll
    for (int i = 0; i < 4; ++i) {
      glds16(ssmk + (size_t)d * S_ + i * 512 + lane * 8, Kd + i * 1024);
      glds16(ws + WS_KVP + (size_t)d * 4096 + i * 1024 + lane * 16, Xd + i * 1024);
    }
    {
      u32x4 z = {0u, 0u, 0u, 0u};
      *(LAS u32x4*)(lb + ((lane < 16) ? lane * 16 : 4352 - 256 + lane * 16)) = z;
    }
    __builtin_amdgcn_s_setprio(3);
    const __bf16 k0 = (__bf16)(bf_lo((unsigned)ssmd[d]) + bf_lo((unsigned)ssmk[(size_t)d * S_]));

    const int q = (lane & 15) >> 2, p = lane & 3;
    const unsigned kof = (unsigned)(256 - 64 * g - 8 * q + 8 * p);
    const int al = (lane >> 2) & 3, c = lane & 3;
    const unsigned xo0 = (unsigned)(5120 + 256 * al + 16 * kv_pos(g, al, c));
    const unsigned xo1 = (unsigned)(5120 + 256 * al + 16 * kv_pos(g, al + 1, c));

    f32x4 acc[8][4];
#pragma unroll
    for (int t = 0; t < 8; ++t)
#pragma unroll
      for (int G = 0; G < 4; ++G) acc[t][G] = (f32x4){0.f, 0.f, 0.f, 0.f};
    const unsigned lbase = (unsigned)(size_t)(LAS char*)lb;
    unsigned ka = lbase + kof, kb = lbase + kof - 32, xa0 = lbase + xo0, xa1 = lbase + xo1;
    asm volatile("" : "+v"(ka), "+v"(kb), "+v"(xa0), "+v"(xa1));
#pragma unroll
    for (int t = 0; t < 8; ++t) asm volatile("" : "+v"(acc[t][0]), "+v"(acc[t][1]), "+v"(acc[t][2]), "+v"(acc[t][3]));
    asm volatile("s_waitcnt vmcnt(0) lgkmcnt(0)" ::: "memory");
    *(LAS u16*)Kd = __builtin_bit_cast(u16, k0);
    asm volatile("" ::: "memory");
    ST2(2);

    u32x4 xf[2][4], kf[2][4];
    auto ldx = [&](int mu) __attribute__((always_inline)) {
#pragma unroll
      for (int G = mu >> 2; G < 4; ++G) xf[mu & 1][G] = lds_rd128(((mu & 1) ? xa1 : xa0) + 256 * (4 * G - mu));
    };
    auto ldk = [&](int st) __attribute__((always_inline)) {
#pragma unroll
      for (int j = 0; j < 4; ++j) {
        u32x2 lo = tr_read_a(ka + 128 * st + 32 * j);
        u32x2 hi = tr_read_a(kb + 128 * st + 32 * j);
        kf[st & 1][j] = (u32x4){lo.x, lo.y, hi.x, hi.y};
      }
    };
    ldx(0);
    ldk(0);
#pragma unroll
    for (int st = 0; st < 32; ++st) {
      const int mu = st >> 1;
      if (st < 31) ldk(st + 1);
      if ((st & 1) == 0 && mu < 15) ldx(mu + 1);
      SBAR();
#pragma unroll
      for (int j = 0; j < 4; ++j)
#pragma unroll
        for (int G = mu >> 2; G < 4; ++G)
          acc[4 * (st & 1) + j][G] = mfma16(xf[mu & 1][G], kf[st & 1][j], acc[4 * (st & 1) + j][G]);
      SBAR();
    }
    asm volatile("s_nop 7\n\ts_nop 7\n\ts_nop 7" ::: "memory");
    ST2(3);
    const int i16 = lane & 15;
    char* ab = smem + 40960 + (128 * g + i16) * 8 + w * 2;
#pragma unroll
    for (int G = 0; G < 4; ++G) {
      float pv[4];
#pragma unroll
      for (int r = 1; r < 4; ++r) {
        float prov = (g == 3) ? ((G > 0) ? acc[7][G > 0 ? G - 1 : 0][r] : 0.f) : acc[7][G][r];
        pv[r] = __shfl(prov, (lane + 48) & 63);
      }
      float y[8];
#pragma unroll
      for (int t = 7; t >= 0; --t) {
        float p1 = (t > 0) ? acc[t > 0 ? t - 1 : 0][G][1] : pv[1];
        float p2 = (t > 0) ? acc[t > 0 ? t - 1 : 0][G][2] : pv[2];
        float p3 = (t > 0) ? acc[t > 0 ? t - 1 : 0][G][3] : pv[3];
        float v = acc[t][G][0];
        asm volatile(
            "s_nop 1\n\t"
            "v_add_f32_dpp %0, %1, %0 row_shr:1 row_mask:0xf bank_mask:0xf bound_ctrl:1\n\t"
            "v_add_f32_dpp %0, %2, %0 row_shr:2 row_mask:0xf bank_mask:0xf bound_ctrl:1\n\t"
            "v_add_f32_dpp %0, %3, %0 row_shr:3 row_mask:0xf bank_mask:0xf bound_ctrl:1\n\t"
            "v_add_f32_dpp %0, %4, %0 row_shl:15 row_mask:0xf bank_mask:0xf bound_ctrl:1\n\t"
            "v_add_f32_dpp %0, %5, %0 row_shl:14 row_mask:0xf bank_mask:0xf bound_ctrl:1\n\t"
            "v_add_f32_dpp %0, %6, %0 row_shl:13 row_mask:0xf bank_mask:0xf bound_ctrl:1"
            : "+v"(v)
            : "v"(acc[t][G][1]), "v"(acc[t][G][2]), "v"(acc[t][G][3]), "v"(p1), "v"(p2), "v"(p3));
        y[t] = v;
      }
#pragma unroll
      for (int t = 0; t < 8; t += 2) {
        unsigned pk = pack_bf16(y[t], y[t + 1]);
        *(LAS u16*)(ab + G * 4096 + t * 128) = (u16)pk;
        *(LAS u16*)(ab + G * 4096 + t * 128 + 128) = (u16)(pk >> 16);
      }
    }
    ST2(4);
    __syncthreads();
#pragma unroll
    for (int i = 0; i < 8; ++i) {
      int s = threadIdx.x + 256 * i;
      unsigned long long v = *(LAS unsigned long long*)(smem + 40960 + s * 8);
      if (v == ~0ull) v = ~1ull;
      size_t o = WS_YT + ((size_t)((s >> 4) * 40 + (cb >> 2)) * 64 + (s & 15) + 16 * (cb & 3)) * 8;
      __hip_atomic_store((GAS unsigned long long*)(ws + o), v, __ATOMIC_RELAXED, __HIP_MEMORY_SCOPE_AGENT);
    }
    ST2(5);
    ST2(6);
    ST2_RT(11);
    ST2_OUT();
    return;
  }

  const int b = blockIdx.x - NCONV;
  const int xcd = b & 7, bi = b >> 3;
  const int mt = (xcd >> 1) * 8 + (bi >> 2);
  const int nt = (xcd & 1) * 4 + (bi & 3);
  const int ksf = (w < 2) ? w * 14 : 27 + (w - 2) * 13;
  const char* hp = ws + WS_HT + (size_t)(mt * 4 * NKS2 + ksf) * 1024;
  const char* wp = ws + WS_WDT + (size_t)(nt * 5 * NKS2 + ksf) * 1024;
  const int m16 = mt * 4 + w;
  const int row = m16 * 16 + (lane & 15);

  u32x2 xv[5], stv;
  {
    const unsigned xo = (unsigned)((row * D_ + nt * 80 + 4 * g) * 2);
    gload8s<0>(xv[0], xo, x);
    gload8s<32>(xv[1], xo, x);
    gload8s<64>(xv[2], xo, x);
    gload8s<96>(xv[3], xo, x);
    gload8s<128>(xv[4], xo, x);
    gload8s<0>(stv, (unsigned)(row * 8), ws + WS_STATS);
  }

  f32x4 acc[4][5];
#pragma unroll
  for (int m = 0; m < 4; ++m)
#pragma unroll
    for (int n = 0; n < 5; ++n) acc[m][n] = (f32x4){0.f, 0.f, 0.f, 0.f};

  u32x4 hf[4][4], wf[4][5];
  unsigned vo[4];
#pragma unroll
  for (int i = 0; i < 4; ++i) vo[i] = (unsigned)(lane * 16 + i * 4096);
#define LOADK(j)                                                                           \
  do {                                                                                     \
    _Pragma("unroll") for (int m = 0; m < 4; ++m)                                          \
        gload16s<((j) & 3) * 1024>(hf[(j) & 3][m], vo[(j) >> 2], hp + (size_t)m * NKS2 * 1024); \
    _Pragma("unroll") for (int n = 0; n < 5; ++n)                                          \
        gload16s<((j) & 3) * 1024>(wf[(j) & 3][n], vo[(j) >> 2], wp + (size_t)n * NKS2 * 1024); \
  } while (0)
#define WAITK(j, N)                                                                        \
  asm volatile("s_waitcnt vmcnt(" #N ")"                                                   \
               : "+v"(hf[(j) & 3][0]), "+v"(hf[(j) & 3][1]), "+v"(hf[(j) & 3][2]), "+v"(hf[(j) & 3][3]), \
                 "+v"(wf[(j) & 3][0]), "+v"(wf[(j) & 3][1]), "+v"(wf[(j) & 3][2]), "+v"(wf[(j) & 3][3]), \
                 "+v"(wf[(j) & 3][4])::"memory")
#define MMAK(j)                                                                            \
  do {                                                                                     \
    _Pragma("unroll") for (int m = 0; m < 4; ++m)                                          \
        _Pragma("unroll") for (int n = 0; n < 5; ++n)                                      \
            acc[m][n] = mfma16(wf[(j) & 3][n], hf[(j) & 3][m], acc[m][n]);                 \
    SBAR();                                                                                \
  } while (0)
  if (w < 2) LOADK(0);
  LOADK(1);
  LOADK(2);
  LOADK(3);
  if (w < 2) { WAITK(0, 27); MMAK(0); }
  LOADK(4);  WAITK(1, 27);  MMAK(1);
  LOADK(5);  WAITK(2, 27);  MMAK(2);
  LOADK(6);  WAITK(3, 27);  MMAK(3);
  LOADK(7);  WAITK(4, 27);  MMAK(4);
  LOADK(8);  WAITK(5, 27);  MMAK(5);
  LOADK(9);  WAITK(6, 27);  MMAK(6);
  LOADK(10); WAITK(7, 27);  MMAK(7);
  LOADK(11); WAITK(8, 27);  MMAK(8);
  LOADK(12); WAITK(9, 27);  MMAK(9);
  LOADK(13); WAITK(10, 27); MMAK(10);
  WAITK(11, 18); MMAK(11);
  WAITK(12, 9);  MMAK(12);
  WAITK(13, 0);  MMAK(13);
  asm volatile("" : "+v"(xv[0]), "+v"(xv[1]), "+v"(xv[2]), "+v"(xv[3]), "+v"(xv[4]), "+v"(stv));
  ST2(3);
#pragma unroll
  for (int m = 0; m < 4; ++m)
#pragma unroll
    for (int n = 0; n < 5; ++n) *(LAS f32x4*)(smem + ((w * 20 + m * 5 + n) * 64 + lane) * 16) = acc[m][n];
  GAS unsigned long long* yp = (GAS unsigned long long*)(ws + WS_YT + ((size_t)(m16 * 40 + nt * 5) * 64 + lane) * 8);
  unsigned long long yv[5];
#pragma unroll
  for (int n = 0; n < 5; ++n) yv[n] = __hip_atomic_load(yp + n * 64, __ATOMIC_RELAXED, __HIP_MEMORY_SCOPE_AGENT);
  __builtin_amdgcn_s_waitcnt(0xC07F);
  __builtin_amdgcn_s_barrier();
  asm volatile("" ::: "memory");
  ST2(4);
  const float mean = __uint_as_float(stv.x), rden = 1.0f / __uint_as_float(stv.y);
  const f32x2 nm2 = {-mean, -mean}, rd2 = {rden, rden}, rs2 = {rden * rden, rden * rden};
  f32x2 ob[5][2], xn[5][2];
#pragma unroll
  for (int n = 0; n < 5; ++n) {
    f32x4 s = *(LAS f32x4*)(smem + ((0 * 20 + w * 5 + n) * 64 + lane) * 16);
#pragma unroll
    for (int ww = 1; ww < 4; ++ww) s += *(LAS f32x4*)(smem + ((ww * 20 + w * 5 + n) * 64 + lane) * 16);
    f32x2 x0 = {bf_lo(xv[n].x), bf_hi(xv[n].x)}, x1 = {bf_lo(xv[n].y), bf_hi(xv[n].y)};
    xn[n][0] = (x0 + nm2) * rd2;
    xn[n][1] = (x1 + nm2) * rd2;
    ob[n][0] = __builtin_elementwise_fma((f32x2){s[0], s[1]}, rs2, x0);
    ob[n][1] = __builtin_elementwise_fma((f32x2){s[2], s[3]}, rs2, x1);
  }
  while (true) {
    bool miss = false;
#pragma unroll
    for (int n = 0; n < 5; ++n) miss |= (yv[n] == ~0ull);
    if (__builtin_amdgcn_ballot_w64(miss) == 0) break;
    __builtin_amdgcn_s_sleep(1);
#pragma unroll
    for (int n = 0; n < 5; ++n) yv[n] = __hip_atomic_load(yp + n * 64, __ATOMIC_RELAXED, __HIP_MEMORY_SCOPE_AGENT);
  }
  ST2(5);
#pragma unroll
  for (int n = 0; n < 5; ++n) {
    unsigned y0 = (unsigned)yv[n], y1 = (unsigned)(yv[n] >> 32);
    f32x2 o0 = __builtin_elementwise_fma(xn[n][0], (f32x2){bf_lo(y0), bf_hi(y0)}, ob[n][0]);
    f32x2 o1 = __builtin_elementwise_fma(xn[n][1], (f32x2){bf_lo(y1), bf_hi(y1)}, ob[n][1]);
    u32x2 ov = {pack_bf16(o0.x, o0.y), pack_bf16(o1.x, o1.y)};
    *(u32x2*)(out + (size_t)row * D_ + (nt * 5 + n) * 16 + 4 * g) = ov;
  }
  ST2(6);
  ST2_RT(11);
  ST2_OUT();
}

extern "C" void kernel_launch(void* const* d_in, const int* in_sizes, int n_in, void* d_out, int out_size, void* d_ws,
                              size_t ws_size, hipStream_t stream) {
  const u16* q = (const u16*)d_in[0];
  const u16* k = (const u16*)d_in[1];
  const u16* v = (const u16*)d_in[2];
  const u16* shk = (const u16*)d_in[3];
  const u16* ssmk = (const u16*)d_in[4];
  const u16* ssmd = (const u16*)d_in[5];
  const u16* wg = (const u16*)d_in[7];
  const u16* wu = (const u16*)d_in[8];
  const u16* wd = (const u16*)d_in[9];
  (void)hipFuncSetAttribute((const void*)k1_kernel, hipFuncAttributeMaxDynamicSharedMemorySize, K1_LDS);
  (void)hipFuncSetAttribute((const void*)k2_kernel, hipFuncAttributeMaxDynamicSharedMemorySize, K2_LDS);
  k1_kernel<<<256, 512, K1_LDS, stream>>>(q, k, v, shk, wg, wu, wd, (char*)d_ws);
  k2_kernel<<<NCONV + 256, 256, K2_LDS, stream>>>(q, ssmk, ssmd, (char*)d_ws, (u16*)d_out);
}
```

```cpp
#include <hip/hip_runtime.h>
#include <hip/hip_bf16.h>
#include <stdint.h>

typedef __bf16 bf16x8 __attribute__((ext_vector_type(8)));
typedef __bf16 bf16x2 __attribute__((ext_vector_type(2)));
typedef short s16x4 __attribute__((ext_vector_type(4)));
typedef float f32x4 __attribute__((ext_vector_type(4)));
typedef float f32x16 __attribute__((ext_vector_type(16)));
typedef float f32x2 __attribute__((ext_vector_type(2)));
typedef unsigned u32x4 __attribute__((ext_vector_type(4)));
typedef unsigned u32x2 __attribute__((ext_vector_type(2)));
typedef unsigned short u16;
#define LAS __attribute__((address_space(3)))
#define GAS __attribute__((address_space(1)))

constexpr int S_ = 2048, D_ = 640, F_ = 1728;
constexpr int NKS2 = 54;

constexpr size_t WS_HT = 0;
constexpr size_t WS_WDT = WS_HT + (size_t)128 * NKS2 * 1024;
constexpr size_t WS_KVP = WS_WDT + (size_t)40 * NKS2 * 1024;
constexpr size_t WS_YT = WS_KVP + (size_t)D_ * 4096;
constexpr size_t WS_STATS = WS_YT + (size_t)S_ * D_ * 2;

__device__ __forceinline__ float bf16r(float x) { return (float)(__bf16)x; }
__device__ __forceinline__ unsigned pack_bf16(float lo, float hi) {
  f32x2 f = {lo, hi};
  return __builtin_bit_cast(unsigned, __builtin_convertvector(f, bf16x2));
}
__device__ __forceinline__ float bf_lo(unsigned u) { return __uint_as_float(u << 16); }
__device__ __forceinline__ float bf_hi(unsigned u) { return __uint_as_float(u & 0xffff0000u); }
__device__ __forceinline__ float dot2(unsigned a, unsigned b, float c) {
  return __builtin_amdgcn_fdot2_f32_bf16(__builtin_bit_cast(bf16x2, a), __builtin_bit_cast(bf16x2, b), c, false);
}
__device__ __forceinline__ f32x4 mfma16(u32x4 a, u32x4 b, f32x4 c) {
  return __builtin_amdgcn_mfma_f32_16x16x32_bf16(__builtin_bit_cast(bf16x8, a), __builtin_bit_cast(bf16x8, b), c, 0, 0, 0);
}
__device__ __forceinline__ u32x2 tr_read(const char* lds_base, unsigned off) {
  s16x4 v = __builtin_amdgcn_ds_read_tr16_b64_v4i16((LAS s16x4*)(lds_base + off));
  return __builtin_bit_cast(u32x2, v);
}
__device__ __forceinline__ void glds16(const void* g, char* l) {
  __builtin_amdgcn_global_load_lds((const GAS unsigned*)g, (LAS unsigned*)l, 16, 0, 0);
}
__device__ __forceinline__ void glds16_asm(const void* g, unsigned lds_dst) {
  unsigned keep;
  asm volatile("s_mov_b32 %0, m0\n\ts_mov_b32 m0, %2\n\ts_nop 0\n\tglobal_load_lds_dwordx4 %1, off\n\ts_mov_b32 m0, %0"
               : "=&s"(keep) : "v"(g), "s"(lds_dst) : "memory");
}
__device__ __forceinline__ u32x2 tr_read_a(unsigned addr) {
  s16x4 v = __builtin_amdgcn_ds_read_tr16_b64_v4i16((LAS s16x4*)addr);
  return __builtin_bit_cast(u32x2, v);
}
__device__ __forceinline__ unsigned kv_pos(unsigned g, unsigned alpha, unsigned c) {
  unsigned f = ((g & 1u) << 1) | ((alpha & 1u) ^ (g >> 1));
  return 4u * f + c;
}

#define ST2_DECL
#define ST2(i)
#define ST2_RT(i)
#define ST2_OUT()
#define STAMP(a, b)
#define STAMP_RT(a, b)
#define STAMP_OUT(a, b)
#define DBG_LDS 0
constexpr int NSTG = 3;
constexpr int STGW = 2 * 64 * 128;
constexpr int STGB = STGW + 256 * 128;
constexpr int K1_CS = NSTG * STGB;
constexpr int K1_FLG = K1_CS + 1536;
constexpr int K1_DBG = K1_FLG + 256;
constexpr int K1_LDS = K1_DBG + DBG_LDS;

#define SBAR() __builtin_amdgcn_sched_barrier(0)
#define BARRIER() do { asm volatile("" ::: "memory"); __builtin_amdgcn_s_barrier(); asm volatile("" ::: "memory"); } while (0)

__device__ __forceinline__ void glds16_saddr(unsigned voff, const void* sbase, unsigned lds_dst) {
  unsigned keep;
  asm volatile("s_mov_b32 %0, m0\n\ts_mov_b32 m0, %3\n\ts_nop 0\n\tglobal_load_lds_dwordx4 %1, %2\n\ts_mov_b32 m0, %0"
               : "=&s"(keep) : "v"(voff), "s"(sbase), "s"(lds_dst) : "memory");
}
__device__ __forceinline__ void lds_signal(unsigned addr) {
  unsigned one = 1u;
  asm volatile("s_mov_b64 exec, 1\n\tds_add_u32 %0, %1\n\ts_mov_b64 exec, -1" ::"v"(addr), "v"(one) : "memory");
}
__device__ __forceinline__ unsigned lds_flag(unsigned addr) { return *(volatile LAS unsigned*)addr; }
#define WACC
#define WARG
__device__ __forceinline__ void lds_wait(unsigned addr, unsigned first, unsigned need WACC) {
  if (__builtin_expect((unsigned)__builtin_amdgcn_readfirstlane(first) < need, 0)) {
    unsigned f;
    do { f = lds_flag(addr); } while ((unsigned)__builtin_amdgcn_readfirstlane(f) < need);
  }
  asm volatile("" ::: "memory");
}
__device__ __forceinline__ u32x4 lds_rd128(unsigned addr) { return *(const LAS u32x4*)addr; }
__device__ __forceinline__ f32x16 mfma32(u32x4 a, u32x4 b, f32x16 c) {
  return __builtin_amdgcn_mfma_f32_32x32x16_bf16(__builtin_bit_cast(bf16x8, a), __builtin_bit_cast(bf16x8, b), c, 0, 0, 0);
}

__device__ __forceinline__ void set_m0(unsigned v) { asm volatile("s_mov_b32 m0, %0\n\ts_nop 0" ::"s"(v) : "memory", "m0"); }
template <int IMM>
__device__ __forceinline__ void glds_i(unsigned voff, const void* sbase) {
  asm volatile("global_load_lds_dwordx4 %0, %1 offset:%2" ::"v"(voff), "s"(sbase), "i"(IMM) : "memory");
}
#define PIN(v) asm volatile("" : "+v"(v))

__global__ __launch_bounds__(512, 2) void k1_kernel(const u16* __restrict__ x, const u16* __restrict__ kin,
                                                    const u16* __restrict__ vin, const u16* __restrict__ shk,
                                                    const u16* __restrict__ wg, const u16* __restrict__ wu,
                                                    const u16* __restrict__ wd, char* __restrict__ ws) {
  extern __shared__ __attribute__((aligned(16))) char smem[];
  const int b = blockIdx.x;
  const int w = __builtin_amdgcn_readfirstlane(threadIdx.x >> 6);
  const int lane = threadIdx.x & 63;
  const unsigned sbase = (unsigned)(size_t)(LAS char*)smem;
  const unsigned flg = sbase + K1_FLG;
  const int xcd = b & 7, bi = b >> 3;
  STAMP_RT(K1_DBG, 0);
  STAMP(K1_DBG, 1);
#define STAMPW(idx)

  if (__builtin_expect(bi >= 27, 0)) {
    const int sw = (xcd * 5 + bi - 27) * 8 + w;
    char* lb = smem + w * 16384;
    const int hs = sw / 10, cgp = sw - hs * 10;
    const int alpha = hs >> 1, half = hs & 1;
    const int c8 = lane & 7, seg = lane >> 3;
    const int s0 = hs * 64 + seg * 8, d0 = cgp * 64 + c8 * 8;
    u32x4 kr[11], vr[8], sw4[4], xr[10];
#pragma unroll
    for (int i = 0; i < 11; ++i) {
      int s = s0 - 3 + i;
      u32x4 t = *(const u32x4*)(kin + (size_t)max(s, 0) * D_ + d0);
      u32x4 z = {0u, 0u, 0u, 0u};
      kr[i] = (i >= 3 || s >= 0) ? t : z;
    }
#pragma unroll
    for (int i = 0; i < 8; ++i) vr[i] = *(const u32x4*)(vin + (size_t)(s0 + i) * D_ + d0);
#pragma unroll
    for (int i = 0; i < 4; ++i) sw4[i] = *(const u32x4*)(shk + (size_t)d0 * 4 + i * 8);
    const int srow = min(sw, 255) * 8 + seg;
#pragma unroll
    for (int i = 0; i < 10; ++i) xr[i] = *(const u32x4*)(x + (size_t)srow * D_ + c8 * 8 + i * 64);
    const bool dowd = sw < 270;
    const int ks = sw / 5, nq = sw - ks * 5;
    if (dowd) {
#pragma unroll
      for (int i = 0; i < 8; ++i) {
        int r = 4 * i + (lane >> 4);
        int ch = ((((lane & 15) >> 1) ^ (r & 7)) << 1) | (lane & 1);
        glds16_asm(wd + (size_t)(ks * 32 + r) * D_ + nq * 128 + ch * 8, (unsigned)(size_t)(LAS char*)(lb + 8192 + i * 1024));
      }
    }
    {
      char* kb = lb + c8 * 1024 + (seg & 3) * 4;
#pragma unroll
      for (int e = 0; e < 8; ++e) {
        unsigned t01 = sw4[e >> 1][(e & 1) * 2], t23 = sw4[e >> 1][(e & 1) * 2 + 1];
        float w0 = bf_lo(t01), w1 = bf_hi(t01), w2 = bf_lo(t23), w3 = bf_hi(t23);
        float kf[11], r[8];
#pragma unroll
        for (int i = 0; i < 11; ++i) { unsigned t = kr[i][e >> 1]; kf[i] = (e & 1) ? bf_hi(t) : bf_lo(t); }
#pragma unroll
        for (int i = 0; i < 8; ++i) {
          unsigned t = vr[i][e >> 1];
          float vv = (e & 1) ? bf_hi(t) : bf_lo(t);
          float a = kf[i] * w3;
          a = fmaf(kf[i + 1], w2, a);
          a = fmaf(kf[i + 2], w1, a);
          a = bf16r(fmaf(kf[i + 3], w0, a));
          r[i] = a * vv;
        }
#pragma unroll
        for (int c = 0; c < 4; ++c)
          *(LAS unsigned*)(kb + e * 128 + (((seg >> 2) * 4 + c + c8) & 7) * 16) = pack_bf16(r[c], r[c + 4]);
      }
      asm volatile("s_waitcnt vmcnt(0)" ::: "memory");
#pragma unroll
      for (int i = 0; i < 8; ++i) {
        int ch = i * 8 + seg;
        u32x4 v = *(LAS u32x4*)(lb + ch * 128 + ((c8 + i) & 7) * 16);
        *(u32x4*)(ws + WS_KVP + ((size_t)(cgp * 64 + ch) * 16 + alpha) * 256 + kv_pos(half * 2 + (c8 >> 2), alpha, c8 & 3) * 16) = v;
      }
    }
    {
      const u32x4 mk = {~0u, ~0u, ~0u, ~0u};
#pragma unroll
      for (int i = 0; i < 8; ++i) *(u32x4*)(ws + WS_YT + (size_t)sw * 8192 + i * 1024 + lane * 16) = mk;
    }
    if (dowd) {
      const int g = lane >> 4, q = (lane & 15) >> 2, p = lane & 3;
      const int r = 16 * (g >> 1) + 4 * (g & 1) + q;
#pragma unroll
      for (int n = 0; n < 8; ++n) {
        const unsigned o = (unsigned)(8192 + r * 256 + ((n ^ (r & 7)) * 32) + p * 8);
        u32x2 lo = tr_read(lb, o);
        u32x2 hi = tr_read(lb, o + 8 * 256);
        u32x4 v = {lo.x, lo.y, hi.x, hi.y};
        *(u32x4*)(ws + WS_WDT + ((size_t)((nq * 8 + n) * NKS2 + ks) * 64 + lane) * 16) = v;
      }
    }
    if (sw < 256) {
      float sm = 0.f;
#pragma unroll
      for (int i = 0; i < 10; ++i)
#pragma unroll
        for (int e = 0; e < 4; ++e) sm += bf_lo(xr[i][e]) + bf_hi(xr[i][e]);
      sm += __shfl_xor(sm, 1);
      sm += __shfl_xor(sm, 2);
      sm += __shfl_xor(sm, 4);
      const float mean = sm * (1.0f / D_);
      float sq = 0.f;
#pragma unroll
      for (int i = 0; i < 10; ++i)
#pragma unroll
        for (int e = 0; e < 4; ++e) {
          float e0 = bf_lo(xr[i][e]) - mean, e1 = bf_hi(xr[i][e]) - mean;
          sq = fmaf(e0, e0, sq);
          sq = fmaf(e1, e1, sq);
        }
      sq += __shfl_xor(sq, 1);
      sq += __shfl_xor(sq, 2);
      sq += __shfl_xor(sq, 4);
      const float var = sq * (1.0f / D_);
      if (c8 == 0) {
        f32x2 st = {bf16r(mean), bf16r(sqrtf(bf16r(bf16r(var) + 1e-5f)))};
        *(f32x2*)(ws + WS_STATS + (size_t)srow * 8) = st;
      }
    }
    STAMP(K1_DBG, 2);
    STAMP_RT(K1_DBG, 31);
    STAMP_OUT(K1_DBG, 0);
    return;
  }

  const int gi = (xcd & 3) * 27 + bi;
  const int fu = gi >> 2;
  const int mt = (xcd >> 2) * 4 + (gi & 3);
  const int koff = 0;
  const unsigned xo0 = (unsigned)((lane >> 3) * (D_ * 2) + (((lane & 7) ^ (lane >> 4)) * 16));
  const unsigned wo0 = (unsigned)((lane >> 3) * (F_ * 2) + (((lane & 7) ^ (((lane >> 3) & 3) * 2)) * 16));
  const char* xs = (const char*)(x + (size_t)(mt * 256) * D_) + koff * 128;
  const char* gs = (const char*)(wg + (size_t)(koff * 64) * F_ + fu * 64);
  const char* us = (const char*)(wu + (size_t)(koff * 64) * F_ + fu * 64);
  {
    const char* xw = xs + w * (32 * D_ * 2);
    set_m0(sbase + STGW + w * 4096);
    glds_i<0>(xo0, xw);
    glds_i<1024>((xo0 ^ 64u) + (8 * D_ * 2 - 1024), xw);
    glds_i<2048>(xo0 + (16 * D_ * 2 - 2048), xw);
    glds_i<3072>((xo0 ^ 64u) + (24 * D_ * 2 - 3072), xw);
    set_m0(sbase + w * 1024);
    glds_i<0>(wo0, gs + w * (8 * F_ * 2));
    set_m0(sbase + 8192 + w * 1024);
    glds_i<0>(wo0 ^ 32u, us + w * (8 * F_ * 2));
  }
  if (w == 7) *(LAS unsigned*)(flg + lane * 4) = 0u;
  __builtin_amdgcn_s_waitcnt(0xC07F);
  SBAR();
  BARRIER();
  SBAR();

  if (w == 7) {
    asm volatile("s_waitcnt vmcnt(0)" ::: "memory");
    lds_signal(flg);
    return;
  }
  if (w >= 4) {
    const int lw = w - 4;
    __builtin_amdgcn_s_setprio(2);
    unsigned xv[32], wv[16];
#pragma unroll
    for (int i = 0; i < 32; ++i) xv[i] = ((i & 1) ? (xo0 ^ 64u) : xo0) + (unsigned)(i * (8 * D_ * 2) - (i & 3) * 1024);
#pragma unroll
    for (int i = 0; i < 16; ++i) wv[i] = ((i & 8) ? (wo0 ^ 32u) : wo0) + (unsigned)((i & 7) * (8 * F_ * 2) - (i & 3) * 1024);
    int kc = koff + 1 + lw;
    int adv = 1 + lw;
    if (kc >= 10) { kc -= 10; adv -= 10; }
    const unsigned lws = sbase + ((lw == 2) ? 0 : (1 + lw) * STGB);
#pragma unroll 1
    for (int s = 1 + lw; s < 10; s += 3) {
      xs += adv * 128;
      gs += adv * (64 * F_ * 2);
      us += adv * (64 * F_ * 2);
      if (s == 3) {
        asm volatile("s_waitcnt vmcnt(0)" ::: "memory");
        lds_signal(flg);
      }
      if (s >= 3) lds_wait(flg + 64 + (s - 3) * 4, 0u, 4u WARG);
#pragma unroll
      for (int j = 0; j < 4; ++j) {
        set_m0(lws + j * 4096);
        const char* src = (j < 2) ? gs : us;
        glds_i<0>(wv[4 * j], src);
        glds_i<1024>(wv[4 * j + 1], src);
        glds_i<2048>(wv[4 * j + 2], src);
        glds_i<3072>(wv[4 * j + 3], src);
      }
#pragma unroll
      for (int j = 0; j < 8; ++j) {
        set_m0(lws + STGW + j * 4096);
        glds_i<0>(xv[4 * j], xs);
        glds_i<1024>(xv[4 * j + 1], xs);
        glds_i<2048>(xv[4 * j + 2], xs);
        glds_i<3072>(xv[4 * j + 3], xs);
      }
      if (s < 3) {
        asm volatile("s_waitcnt vmcnt(48)" ::: "memory");
        lds_signal(flg);
      }
      STAMP(K1_DBG, 13 + s);
      asm volatile("s_waitcnt vmcnt(0)" ::: "memory");
      lds_signal(flg + s * 4);
      STAMP(K1_DBG, 4 + s);
      adv = (kc >= 7) ? -7 : 3;
      kc += adv;
    }
    STAMPW(26);
    STAMP(K1_DBG, 2);
    STAMP_RT(K1_DBG, 31);
    STAMP_OUT(K1_DBG, 0);
    return;
  }

  __builtin_amdgcn_s_setprio(3);
  const int hi = lane >> 5, q = (lane & 15) >> 2, p = lane & 3, mat = (lane >> 4) & 1;
  const int r2 = w >> 1, c2 = w & 1;
  unsigned roff[2];
#pragma unroll
  for (int j = 0; j < 2; ++j)
    roff[j] = sbase + (unsigned)(mat * 8192 + (8 * hi + q) * 128 + (((2 * c2 + (j ^ r2)) ^ q ^ mat) * 32) + p * 8);
  unsigned aoff[2][4];
#pragma unroll
  for (int kq = 0; kq < 4; ++kq) {
    unsigned o = sbase + (unsigned)(STGW + r2 * 16384 + (lane & 31) * 128 + (((2 * kq + hi) ^ ((lane >> 1) & 7)) * 16));
    aoff[0][kq] = o + c2 * 8192;
    aoff[1][kq] = o + (1 - c2) * 8192;
  }

  f32x16 acc[2][4];
#pragma unroll
  for (int c = 0; c < 8; ++c)
#pragma unroll
    for (int e = 0; e < 16; ++e) acc[c >> 2][c & 3][e] = 0.f;
  f32x4 accc = {0.f, 0.f, 0.f, 0.f}, accs[2] = {{0.f, 0.f, 0.f, 0.f}, {0.f, 0.f, 0.f, 0.f}};
  const unsigned sv = ((lane & 15) == mat) ? 0x3f803f80u : 0u;
  u32x4 sel = {sv, sv, sv, sv};
  asm volatile("" : "+v"(sel), "+v"(accc), "+v"(accs[0]), "+v"(accs[1]), "+v"(roff[0]), "+v"(roff[1]));
#pragma unroll
  for (int c = 0; c < 8; ++c) asm volatile("" : "+v"(acc[c >> 2][c & 3]));
#pragma unroll
  for (int c = 0; c < 4; ++c) asm volatile("" : "+v"(aoff[0][c]), "+v"(aoff[1][c]));
  unsigned fa = flg;
  STAMP(K1_DBG, 2);
  asm volatile("s_waitcnt vmcnt(0)" ::: "memory");
  lds_signal(fa);
  lds_wait(fa, 0u, 8u WARG);
  STAMP(K1_DBG, 3);
  STAMPW(25);

#define RA_ 12
#ifndef FLT_
#define FLT_ 24
#endif
  constexpr int RA = RA_;
#define HASF(j) ((j) != 5 && (j) != 7)
  auto ldf = [&](int tn) __attribute__((always_inline)) -> u32x4 {
    const int kn = (tn >> 3) & 3, jn = tn & 7;
    if ((jn & 1) == 0) return lds_rd128(aoff[jn >> 2][kn] + ((jn >> 1) & 1) * 4096);
    u32x2 lo = tr_read_a(roff[jn >> 1] + kn * 2048);
    u32x2 hh = tr_read_a(roff[jn >> 1] + kn * 2048 + 512);
    return (u32x4){lo.x, lo.y, hh.x, hh.y};
  };
  u32x4 cw[RA];
#pragma unroll
  for (int c = 0; c < RA; ++c) {
    if (HASF(c & 7)) cw[c] = ldf(c);
    SBAR();
  }
  int slot = 0;
#pragma unroll 1
  for (int s = 0; s < 10; ++s) {
    const unsigned dl = (slot == 2) ? (unsigned)(-2 * STGB) : (unsigned)STGB;
    slot = (slot == 2) ? 0 : slot + 1;
    const unsigned need = (s < 9) ? 1u : 0u;
    u32x4 fr[32 + RA];
    unsigned fl = 0u;
#pragma unroll
    for (int c = 0; c < RA; ++c)
      if (HASF(c & 7)) fr[c] = cw[c];
#pragma unroll
    for (int t = 0; t < 32; ++t) {
      const int kq = t >> 3, m = t & 7;
      const int tn = t + RA, kn = (tn >> 3) & 3, jn = tn & 7;
      if (tn == 32) lds_wait(fa + 4, fl, need WARG);
      if (HASF(jn)) {
        fr[tn] = ldf(tn);
        if (tn < 32) {
          if (jn == 2 || jn == 6) { aoff[jn >> 2][kn] += dl; PIN(aoff[jn >> 2][kn]); }
          else if ((jn & 1) && kn == 3) { roff[jn >> 1] += dl; PIN(roff[jn >> 1]); }
        }
      }
      if (t < RA && (m == 2 || m == 6)) { aoff[m >> 2][kq] += dl; PIN(aoff[m >> 2][kq]); }
      if (tn == 30) lds_signal(fa + 64);
      if (tn == FLT_) fl = lds_flag(fa + 4);
      SBAR();
      {
        const int tp = (m < 4) ? (m >> 1) : (m & 1);
        const int sp = (m < 4) ? (m & 1) : (m >> 1);
        acc[tp][sp] = mfma32(fr[8 * kq + 1 + 2 * tp], fr[8 * kq + 2 * sp], acc[tp][sp]);
        if (m == 3) {
          accc = mfma16(fr[8 * kq + 1], sel, accc);
          accs[0] = mfma16(sel, fr[8 * kq], accs[0]);
          accs[1] = mfma16(sel, fr[8 * kq + 2], accs[1]);
        }
      }
      SBAR();
    }
#pragma unroll
    for (int c = 0; c < RA; ++c)
      if (HASF(c & 7)) cw[c] = fr[32 + c];
    fa += 4;
    STAMP(K1_DBG, 4 + s);
  }
  STAMPW(26);

  STAMP(K1_DBG, 23);
  float* csb = (float*)(smem + K1_CS);
  if ((lane & 15) < 2) *(f32x4*)(csb + (2 * c2 + r2) * 32 + (lane & 15) * 16 + 4 * (lane >> 4)) = accc;
  float nm[4];
#pragma unroll
  for (int i = 0; i < 2; ++i) {
    float v0 = __shfl(accs[i][0], lane & 15), v1 = __shfl(accs[i][1], lane & 15);
    nm[i] = -bf16r(((lane & 16) ? v1 : v0) * (1.0f / D_));
  }
  csb[128 + r2 * 128 + c2 * 64 + lane] = hi ? nm[1] : nm[0];
  lds_signal(flg + 128);
  lds_wait(flg + 128, 0u, 4u WARG);
  nm[2] = csb[128 + r2 * 128 + (1 - c2) * 64 + (lane & 31)];
  nm[3] = csb[128 + r2 * 128 + (1 - c2) * 64 + 32 + (lane & 31)];
  char* hb = ws + WS_HT + ((size_t)((mt * 16 + r2 * 8 + mat) * NKS2 + 2 * fu + c2) * 64 + (lane & 15) + 16 * hi) * 16;
#pragma unroll
  for (int j = 0; j < 2; ++j) {
    const int tb = j ^ r2;
    const float* cp = csb + (2 * c2 + tb) * 32 + 4 * hi;
    f32x4 c4[4] = {*(const f32x4*)(cp), *(const f32x4*)(cp + 8), *(const f32x4*)(cp + 16), *(const f32x4*)(cp + 24)};
#pragma unroll
    for (int u = 0; u < 4; ++u) {
      const f32x2 nm2 = {nm[u], nm[u]};
      u32x4 o;
#pragma unroll
      for (int i = 0; i < 4; ++i) {
        f32x2 gv = {acc[j][u][2 * i], acc[j][u][2 * i + 1]}, uv = {acc[j][u][8 + 2 * i], acc[j][u][9 + 2 * i]};
        f32x2 cgv = {c4[i >> 1][(2 * i) & 3], c4[i >> 1][(2 * i + 1) & 3]};
        f32x2 cuv = {c4[2 + (i >> 1)][(2 * i) & 3], c4[2 + (i >> 1)][(2 * i + 1) & 3]};
        gv = __builtin_elementwise_fma(nm2, cgv, gv);
        uv = __builtin_elementwise_fma(nm2, cuv, uv);
        f32x2 hv = gv * uv;
        o[i] = pack_bf16(hv.x, hv.y);
      }
      *(u32x4*)(hb + (size_t)((u ^ (2 * c2)) * 2 * NKS2) * 1024 + tb * 512) = o;
    }
  }
  STAMP(K1_DBG, 24);
  STAMP_RT(K1_DBG, 31);
  STAMP_OUT(K1_DBG, 0);
}

constexpr int K2_LDS = 4 * 20 * 1024;
constexpr int NCONV = 160;

template <int OFF>
__device__ __forceinline__ void gload16s(u32x4& dst, unsigned voff, const void* sbase) {
  asm volatile("global_load_dwordx4 %0, %1, %2 offset:%3" : "=v"(dst) : "v"(voff), "s"(sbase), "i"(OFF) : "memory");
}
template <int OFF>
__device__ __forceinline__ void gload8s(u32x2& dst, unsigned voff, const void* sbase) {
  asm volatile("global_load_dwordx2 %0, %1, %2 offset:%3" : "=v"(dst) : "v"(voff), "s"(sbase), "i"(OFF) : "memory");
}
template <int N>
__device__ __forceinline__ float dpp_shr(float v) {
  return __builtin_bit_cast(float, __builtin_amdgcn_update_dpp(0, __builtin_bit_cast(int, v), 0x110 + N, 0xf, 0xf, true));
}
template <int N>
__device__ __forceinline__ float dpp_shl(float v) {
  return __builtin_bit_cast(float, __builtin_amdgcn_update_dpp(0, __builtin_bit_cast(int, v), 0x100 + N, 0xf, 0xf, true));
}

__global__ __launch_bounds__(256, 2) void k2_kernel(const u16* __restrict__ x, const u16* __restrict__ ssmk,
                                                    const u16* __restrict__ ssmd, char* ws, u16* __restrict__ out) {
  extern __shared__ __attribute__((aligned(16))) char smem[];
  const int w = __builtin_amdgcn_readfirstlane(threadIdx.x >> 6);
  const int lane = threadIdx.x & 63;
  const int g = lane >> 4;
  ST2_DECL;
  ST2_RT(0);
  ST2(1);

  if (blockIdx.x < NCONV) {
    const int cb = blockIdx.x;
    const int d = cb * 4 + w;
    char* lb = smem + w * 9216;
    char* Kd = lb + 256;
    char* Xd = lb + 5120;
#pragma unroll
    for (int i = 0; i < 4; ++i) {
      glds16(ssmk + (size_t)d * S_ + i * 512 + lane * 8, Kd + i * 1024);
      glds16(ws + WS_KVP + (size_t)d * 4096 + i * 1024 + lane * 16, Xd + i * 1024);
    }
    {
      u32x4 z = {0u, 0u, 0u, 0u};
      *(LAS u32x4*)(lb + ((lane < 16) ? lane * 16 : 4352 - 256 + lane * 16)) = z;
    }
    __builtin_amdgcn_s_setprio(3);
    const __bf16 k0 = (__bf16)(bf_lo((unsigned)ssmd[d]) + bf_lo((unsigned)ssmk[(size_t)d * S_]));

    const int q = (lane & 15) >> 2, p = lane & 3;
    const unsigned kof = (unsigned)(256 - 64 * g - 8 * q + 8 * p);
    const int al = (lane >> 2) & 3, c = lane & 3;
    const unsigned xo0 = (unsigned)(5120 + 256 * al + 16 * kv_pos(g, al, c));
    const unsigned xo1 = (unsigned)(5120 + 256 * al + 16 * kv_pos(g, al + 1, c));

    f32x4 acc[8][4];
#pragma unroll
    for (int t = 0; t < 8; ++t)
#pragma unroll
      for (int G = 0; G < 4; ++G) acc[t][G] = (f32x4){0.f, 0.f, 0.f, 0.f};
    const unsigned lbase = (unsigned)(size_t)(LAS char*)lb;
    unsigned ka = lbase + kof, kb = lbase + kof - 32, xa0 = lbase + xo0, xa1 = lbase + xo1;
    asm volatile("" : "+v"(ka), "+v"(kb), "+v"(xa0), "+v"(xa1));
#pragma unroll
    for (int t = 0; t < 8; ++t) asm volatile("" : "+v"(acc[t][0]), "+v"(acc[t][1]), "+v"(acc[t][2]), "+v"(acc[t][3]));
    asm volatile("s_waitcnt vmcnt(0) lgkmcnt(0)" ::: "memory");
    *(LAS u16*)Kd = __builtin_bit_cast(u16, k0);
    asm volatile("" ::: "memory");
    ST2(2);

    u32x4 xf[2][4], kf[2][4];
    auto ldx = [&](int mu) __attribute__((always_inline)) {
#pragma unroll
      for (int G = mu >> 2; G < 4; ++G) xf[mu & 1][G] = lds_rd128(((mu & 1) ? xa1 : xa0) + 256 * (4 * G - mu));
    };
    auto ldk = [&](int st) __attribute__((always_inline)) {
#pragma unroll
      for (int j = 0; j < 4; ++j) {
        u32x2 lo = tr_read_a(ka + 128 * st + 32 * j);
        u32x2 hi = tr_read_a(kb + 128 * st + 32 * j);
        kf[st & 1][j] = (u32x4){lo.x, lo.y, hi.x, hi.y};
      }
    };
    ldx(0);
    ldk(0);
#pragma unroll
    for (int st = 0; st < 32; ++st) {
      const int mu = st >> 1;
      if (st < 31) ldk(st + 1);
      if ((st & 1) == 0 && mu < 15) ldx(mu + 1);
      SBAR();
#pragma unroll
      for (int j = 0; j < 4; ++j)
#pragma unroll
        for (int G = mu >> 2; G < 4; ++G)
          acc[4 * (st & 1) + j][G] = mfma16(xf[mu & 1][G], kf[st & 1][j], acc[4 * (st & 1) + j][G]);
      SBAR();
    }
    asm volatile("s_nop 7\n\ts_nop 7\n\ts_nop 7" ::: "memory");
    ST2(3);
    const int i16 = lane & 15;
    char* ab = smem + 40960 + (128 * g + i16) * 8 + w * 2;
#pragma unroll
    for (int G = 0; G < 4; ++G) {
      float pv[4];
#pragma unroll
      for (int r = 1; r < 4; ++r) {
        float prov = (g == 3) ? ((G > 0) ? acc[7][G > 0 ? G - 1 : 0][r] : 0.f) : acc[7][G][r];
        pv[r] = __shfl(prov, (lane + 48) & 63);
      }
      float y[8];
#pragma unroll
      for (int t = 7; t >= 0; --t) {
        float p1 = (t > 0) ? acc[t > 0 ? t - 1 : 0][G][1] : pv[1];
        float p2 = (t > 0) ? acc[t > 0 ? t - 1 : 0][G][2] : pv[2];
        float p3 = (t > 0) ? acc[t > 0 ? t - 1 : 0][G][3] : pv[3];
        float v = acc[t][G][0];
        asm volatile(
            "s_nop 1\n\t"
            "v_add_f32_dpp %0, %1, %0 row_shr:1 row_mask:0xf bank_mask:0xf bound_ctrl:1\n\t"
            "v_add_f32_dpp %0, %2, %0 row_shr:2 row_mask:0xf bank_mask:0xf bound_ctrl:1\n\t"
            "v_add_f32_dpp %0, %3, %0 row_shr:3 row_mask:0xf bank_mask:0xf bound_ctrl:1\n\t"
            "v_add_f32_dpp %0, %4, %0 row_shl:15 row_mask:0xf bank_mask:0xf bound_ctrl:1\n\t"
            "v_add_f32_dpp %0, %5, %0 row_shl:14 row_mask:0xf bank_mask:0xf bound_ctrl:1\n\t"
            "v_add_f32_dpp %0, %6, %0 row_shl:13 row_mask:0xf bank_mask:0xf bound_ctrl:1"
            : "+v"(v)
            : "v"(acc[t][G][1]), "v"(acc[t][G][2]), "v"(acc[t][G][3]), "v"(p1), "v"(p2), "v"(p3));
        y[t] = v;
      }
#pragma unroll
      for (int t = 0; t < 8; t += 2) {
        unsigned pk = pack_bf16(y[t], y[t + 1]);
        *(LAS u16*)(ab + G * 4096 + t * 128) = (u16)pk;
        *(LAS u16*)(ab + G * 4096 + t * 128 + 128) = (u16)(pk >> 16);
      }
    }
    ST2(4);
    __syncthreads();
#pragma unroll
    for (int i = 0; i < 8; ++i) {
      int s = threadIdx.x + 256 * i;
      unsigned long long v = *(LAS unsigned long long*)(smem + 40960 + s * 8);
      if (v == ~0ull) v = ~1ull;
      size_t o = WS_YT + ((size_t)((s >> 4) * 40 + (cb >> 2)) * 64 + (s & 15) + 16 * (cb & 3)) * 8;
      __hip_atomic_store((GAS unsigned long long*)(ws + o), v, __ATOMIC_RELAXED, __HIP_MEMORY_SCOPE_AGENT);
    }
    ST2(5);
    ST2(6);
    ST2_RT(11);
    ST2_OUT();
    return;
  }

  const int b = blockIdx.x - NCONV;
  const int xcd = b & 7, bi = b >> 3;
  const int mt = (xcd >> 1) * 8 + (bi >> 2);
  const int nt = (xcd & 1) * 4 + (bi & 3);
  const int ksf = (w < 2) ? w * 14 : 27 + (w - 2) * 13;
  const char* hp = ws + WS_HT + (size_t)(mt * 4 * NKS2 + ksf) * 1024;
  const char* wp = ws + WS_WDT + (size_t)(nt * 5 * NKS2 + ksf) * 1024;
  const int m16 = mt * 4 + w;
  const int row = m16 * 16 + (lane & 15);

  u32x2 xv[5], stv;
  {
    const unsigned xo = (unsigned)((row * D_ + nt * 80 + 4 * g) * 2);
    gload8s<0>(xv[0], xo, x);
    gload8s<32>(xv[1], xo, x);
    gload8s<64>(xv[2], xo, x);
    gload8s<96>(xv[3], xo, x);
    gload8s<128>(xv[4], xo, x);
    gload8s<0>(stv, (unsigned)(row * 8), ws + WS_STATS);
  }

  f32x4 acc[4][5];
#pragma unroll
  for (int m = 0; m < 4; ++m)
#pragma unroll
    for (int n = 0; n < 5; ++n) acc[m][n] = (f32x4){0.f, 0.f, 0.f, 0.f};

  u32x4 hf[4][4], wf[4][5];
  unsigned vo[4];
#pragma unroll
  for (int i = 0; i < 4; ++i) vo[i] = (unsigned)(lane * 16 + i * 4096);
#define LOADK(j)                                                                           \
  do {                                                                                     \
    _Pragma("unroll") for (int m = 0; m < 4; ++m)                                          \
        gload16s<((j) & 3) * 1024>(hf[(j) & 3][m], vo[(j) >> 2], hp + (size_t)m * NKS2 * 1024); \
    _Pragma("unroll") for (int n = 0; n < 5; ++n)                                          \
        gload16s<((j) & 3) * 1024>(wf[(j) & 3][n], vo[(j) >> 2], wp + (size_t)n * NKS2 * 1024); \
  } while (0)
#define WAITK(j, N)                                                                        \
  asm volatile("s_waitcnt vmcnt(" #N ")"                                                   \
               : "+v"(hf[(j) & 3][0]), "+v"(hf[(j) & 3][1]), "+v"(hf[(j) & 3][2]), "+v"(hf[(j) & 3][3]), \
                 "+v"(wf[(j) & 3][0]), "+v"(wf[(j) & 3][1]), "+v"(wf[(j) & 3][2]), "+v"(wf[(j) & 3][3]), \
                 "+v"(wf[(j) & 3][4])::"memory")
#define MMAK(j)                                                                            \
  do {                                                                                     \
    _Pragma("unroll") for (int m = 0; m < 4; ++m)                                          \
        _Pragma("unroll") for (int n = 0; n < 5; ++n)                                      \
            acc[m][n] = mfma16(wf[(j) & 3][n], hf[(j) & 3][m], acc[m][n]);                 \
    SBAR();                                                                                \
  } while (0)
  if (w < 2) LOADK(0);
  LOADK(1);
  LOADK(2);
  LOADK(3);
  if (w < 2) { WAITK(0, 27); MMAK(0); }
  LOADK(4);  WAITK(1, 27);  MMAK(1);
  LOADK(5);  WAITK(2, 27);  MMAK(2);
  LOADK(6);  WAITK(3, 27);  MMAK(3);
  LOADK(7);  WAITK(4, 27);  MMAK(4);
  LOADK(8);  WAITK(5, 27);  MMAK(5);
  LOADK(9);  WAITK(6, 27);  MMAK(6);
  LOADK(10); WAITK(7, 27);  MMAK(7);
  LOADK(11); WAITK(8, 27);  MMAK(8);
  LOADK(12); WAITK(9, 27);  MMAK(9);
  LOADK(13); WAITK(10, 27); MMAK(10);
  WAITK(11, 18); MMAK(11);
  WAITK(12, 9);  MMAK(12);
  WAITK(13, 0);  MMAK(13);
  asm volatile("" : "+v"(xv[0]), "+v"(xv[1]), "+v"(xv[2]), "+v"(xv[3]), "+v"(xv[4]), "+v"(stv));
  ST2(3);
#pragma unroll
  for (int m = 0; m < 4; ++m)
#pragma unroll
    for (int n = 0; n < 5; ++n) *(LAS f32x4*)(smem + ((w * 20 + m * 5 + n) * 64 + lane) * 16) = acc[m][n];
  GAS unsigned long long* yp = (GAS unsigned long long*)(ws + WS_YT + ((size_t)(m16 * 40 + nt * 5) * 64 + lane) * 8);
  unsigned long long yv[5];
#pragma unroll
  for (int n = 0; n < 5; ++n) yv[n] = __hip_atomic_load(yp + n * 64, __ATOMIC_RELAXED, __HIP_MEMORY_SCOPE_AGENT);
  __builtin_amdgcn_s_waitcnt(0xC07F);
  __builtin_amdgcn_s_barrier();
  asm volatile("" ::: "memory");
  ST2(4);
  const float mean = __uint_as_float(stv.x), rden = 1.0f / __uint_as_float(stv.y);
  const f32x2 nm2 = {-mean, -mean}, rd2 = {rden, rden}, rs2 = {rden * rden, rden * rden};
  f32x2 ob[5][2], xn[5][2];
#pragma unroll
  for (int n = 0; n < 5; ++n) {
    f32x4 s = *(LAS f32x4*)(smem + ((0 * 20 + w * 5 + n) * 64 + lane) * 16);
#pragma unroll
    for (int ww = 1; ww < 4; ++ww) s += *(LAS f32x4*)(smem + ((ww * 20 + w * 5 + n) * 64 + lane) * 16);
    f32x2 x0 = {bf_lo(xv[n].x), bf_hi(xv[n].x)}, x1 = {bf_lo(xv[n].y), bf_hi(xv[n].y)};
    xn[n][0] = (x0 + nm2) * rd2;
    xn[n][1] = (x1 + nm2) * rd2;
    ob[n][0] = __builtin_elementwise_fma((f32x2){s[0], s[1]}, rs2, x0);
    ob[n][1] = __builtin_elementwise_fma((f32x2){s[2], s[3]}, rs2, x1);
  }
  while (true) {
    bool miss = false;
#pragma unroll
    for (int n = 0; n < 5; ++n) miss |= (yv[n] == ~0ull);
    if (__builtin_amdgcn_ballot_w64(miss) == 0) break;
    __builtin_amdgcn_s_sleep(1);
#pragma unroll
    for (int n = 0; n < 5; ++n) yv[n] = __hip_atomic_load(yp + n * 64, __ATOMIC_RELAXED, __HIP_MEMORY_SCOPE_AGENT);
  }
  ST2(5);
#pragma unroll
  for (int n = 0; n < 5; ++n) {
    unsigned y0 = (unsigned)yv[n], y1 = (unsigned)(yv[n] >> 32);
    f32x2 o0 = __builtin_elementwise_fma(xn[n][0], (f32x2){bf_lo(y0), bf_hi(y0)}, ob[n][0]);
    f32x2 o1 = __builtin_elementwise_fma(xn[n][1], (f32x2){bf_lo(y1), bf_hi(y1)}, ob[n][1]);
    u32x2 ov = {pack_bf16(o0.x, o0.y), pack_bf16(o1.x, o1.y)};
    *(u32x2*)(out + (size_t)row * D_ + (nt * 5 + n) * 16 + 4 * g) = ov;
  }
  ST2(6);
  ST2_RT(11);
  ST2_OUT();
}

extern "C" void kernel_launch(void* const* d_in, const int* in_sizes, int n_in, void* d_out, int out_size, void* d_ws,
                              size_t ws_size, hipStream_t stream) {
  const u16* q = (const u16*)d_in[0];
  const u16* k = (const u16*)d_in[1];
  const u16* v = (const u16*)d_in[2];
  const u16* shk = (const u16*)d_in[3];
  const u16* ssmk = (const u16*)d_in[4];
  const u16* ssmd = (const u16*)d_in[5];
  const u16* wg = (const u16*)d_in[7];
  const u16* wu = (const u16*)d_in[8];
  const u16* wd = (const u16*)d_in[9];
  (void)hipFuncSetAttribute((const void*)k1_kernel, hipFuncAttributeMaxDynamicSharedMemorySize, K1_LDS);
  (void)hipFuncSetAttribute((const void*)k2_kernel, hipFuncAttributeMaxDynamicSharedMemorySize, K2_LDS);
  k1_kernel<<<256, 512, K1_LDS, stream>>>(q, k, v, shk, wg, wu, wd, (char*)d_ws);
  k2_kernel<<<NCONV + 256, 256, K2_LDS, stream>>>(q, ssmk, ssmd, (char*)d_ws, (u16*)d_out);
}
```

```cpp
#define CSW 8
#define MPOLL 1
#define PSL 1
#define CPRIO 0
#define CPRIO2 3
#define GPRIO 2
#define OUTV 2
#define POLO " sc1"
#define KV_ST 14
#define MK_ST 3
#define ET 1
#define NOCT2
#define TOPT 2
#define SWD 0
#define SD2 64
#define NWT (TOPT == 2 ? 378 : 108)
#include <hip/hip_runtime.h>
#include <hip/hip_bf16.h>
#include <stdint.h>

typedef __bf16 bf16x8 __attribute__((ext_vector_type(8)));
typedef __bf16 bf16x2 __attribute__((ext_vector_type(2)));
typedef short s16x4 __attribute__((ext_vector_type(4)));
typedef float f32x4 __attribute__((ext_vector_type(4)));
typedef float f32x16 __attribute__((ext_vector_type(16)));
typedef float f32x2 __attribute__((ext_vector_type(2)));
typedef unsigned u32x4 __attribute__((ext_vector_type(4)));
typedef unsigned u32x2 __attribute__((ext_vector_type(2)));
typedef unsigned short u16;
#define LAS __attribute__((address_space(3)))
#define GAS __attribute__((address_space(1)))

constexpr int S_ = 2048, D_ = 640, F_ = 1728;
constexpr int NKS2 = 54;

constexpr size_t WS_HT = 0;
constexpr size_t WS_WDT = WS_HT + (size_t)128 * NKS2 * 1024;
constexpr size_t WS_KVP = WS_WDT + (size_t)40 * NKS2 * 1024;
constexpr size_t WS_YT = WS_KVP + (size_t)D_ * 4096;
constexpr size_t WS_STATS = WS_YT + (size_t)S_ * D_ * 2;
constexpr size_t WS_K0 = WS_STATS + (size_t)S_ * 8;

__device__ __forceinline__ float bf16r(float x) { return (float)(__bf16)x; }
__device__ __forceinline__ unsigned pack_bf16(float lo, float hi) {
  f32x2 f = {lo, hi};
  return __builtin_bit_cast(unsigned, __builtin_convertvector(f, bf16x2));
}
__device__ __forceinline__ float bf_lo(unsigned u) { return __uint_as_float(u << 16); }
__device__ __forceinline__ float bf_hi(unsigned u) { return __uint_as_float(u & 0xffff0000u); }
__device__ __forceinline__ float dot2(unsigned a, unsigned b, float c) {
  return __builtin_amdgcn_fdot2_f32_bf16(__builtin_bit_cast(bf16x2, a), __builtin_bit_cast(bf16x2, b), c, false);
}
__device__ __forceinline__ f32x4 mfma16(u32x4 a, u32x4 b, f32x4 c) {
  return __builtin_amdgcn_mfma_f32_16x16x32_bf16(__builtin_bit_cast(bf16x8, a), __builtin_bit_cast(bf16x8, b), c, 0, 0, 0);
}
__device__ __forceinline__ u32x2 tr_read(const char* lds_base, unsigned off) {
  s16x4 v = __builtin_amdgcn_ds_read_tr16_b64_v4i16((LAS s16x4*)(lds_base + off));
  return __builtin_bit_cast(u32x2, v);
}
__device__ __forceinline__ void glds16(const void* g, char* l) {
  __builtin_amdgcn_global_load_lds((const GAS unsigned*)g, (LAS unsigned*)l, 16, 0, 0);
}
__device__ __forceinline__ void glds16_asm(const void* g, unsigned lds_dst) {
  unsigned keep;
  asm volatile("s_mov_b32 %0, m0\n\ts_mov_b32 m0, %2\n\ts_nop 0\n\tglobal_load_lds_dwordx4 %1, off\n\ts_mov_b32 m0, %0"
               : "=&s"(keep) : "v"(g), "s"(lds_dst) : "memory");
}
__device__ __forceinline__ u32x2 tr_read_a(unsigned addr) {
  s16x4 v = __builtin_amdgcn_ds_read_tr16_b64_v4i16((LAS s16x4*)addr);
  return __builtin_bit_cast(u32x2, v);
}
__device__ __forceinline__ unsigned kv_pos(unsigned g, unsigned alpha, unsigned c) { return 4u * g + c; }

#define ST2_DECL
#define ST2(i)
#define ST2_RT(i)
#define ST2_OUT()
#define STAMP(a, b)
#define STAMP_RT(a, b)
#define STAMP_OUT(a, b)
#define DBG_LDS 0
constexpr int NSTG = 3;
constexpr int STGW = 2 * 64 * 128;
constexpr int STGB = STGW + 256 * 128;
constexpr int K1_CS = NSTG * STGB;
constexpr int K1_FLG = K1_CS + 1536;
constexpr int K1_KV = K1_FLG + 256;
constexpr int K1_SCR = K1_KV + 10240;
constexpr int K1_DBG = K1_SCR + 256;
constexpr int K1_LDS = K1_DBG + DBG_LDS;

#define SBAR() __builtin_amdgcn_sched_barrier(0)
#define BARRIER() do { asm volatile("" ::: "memory"); __builtin_amdgcn_s_barrier(); asm volatile("" ::: "memory"); } while (0)

__device__ __forceinline__ void glds16_saddr(unsigned voff, const void* sbase, unsigned lds_dst) {
  unsigned keep;
  asm volatile("s_mov_b32 %0, m0\n\ts_mov_b32 m0, %3\n\ts_nop 0\n\tglobal_load_lds_dwordx4 %1, %2\n\ts_mov_b32 m0, %0"
               : "=&s"(keep) : "v"(voff), "s"(sbase), "s"(lds_dst) : "memory");
}
__device__ __forceinline__ void lds_signal(unsigned addr) {
  unsigned one = 1u;
  asm volatile("s_mov_b64 exec, 1\n\tds_add_u32 %0, %1\n\ts_mov_b64 exec, -1" ::"v"(addr), "v"(one) : "memory");
}
__device__ __forceinline__ unsigned lds_flag(unsigned addr) { return *(volatile LAS unsigned*)addr; }
#define WACC
#define WARG
__device__ __forceinline__ void lds_wait(unsigned addr, unsigned first, unsigned need WACC) {
  if (__builtin_expect((unsigned)__builtin_amdgcn_readfirstlane(first) < need, 0)) {
    unsigned f;
    do { f = lds_flag(addr); } while ((unsigned)__builtin_amdgcn_readfirstlane(f) < need);
  }
  asm volatile("" ::: "memory");
}
#define LSLEEP 1
__device__ __forceinline__ void lds_wait_sleep(unsigned addr, unsigned need) {
  while ((unsigned)__builtin_amdgcn_readfirstlane(lds_flag(addr)) < need) __builtin_amdgcn_s_sleep(LSLEEP);
  asm volatile("" ::: "memory");
}
__device__ __forceinline__ u32x4 lds_rd128(unsigned addr) { return *(const LAS u32x4*)addr; }
__device__ __forceinline__ f32x16 mfma32(u32x4 a, u32x4 b, f32x16 c) {
  return __builtin_amdgcn_mfma_f32_32x32x16_bf16(__builtin_bit_cast(bf16x8, a), __builtin_bit_cast(bf16x8, b), c, 0, 0, 0);
}

__device__ __forceinline__ void set_m0(unsigned v) { asm volatile("s_mov_b32 m0, %0\n\ts_nop 0" ::"s"(v) : "memory", "m0"); }
template <int IMM>
__device__ __forceinline__ void glds_i(unsigned voff, const void* sbase) {
  asm volatile("global_load_lds_dwordx4 %0, %1 offset:%2" ::"v"(voff), "s"(sbase), "i"(IMM) : "memory");
}
__device__ __forceinline__ void touch(unsigned& dst, unsigned voff, const void* sbase) {
  asm volatile("global_load_dword %0, %1, %2" : "=v"(dst) : "v"(voff), "s"(sbase) : "memory");
}
__device__ __forceinline__ void touch_l(unsigned voff, const void* sbase, unsigned lds) {
  asm volatile("s_mov_b32 m0, %2\n\ts_nop 0\n\tglobal_load_lds_dword %0, %1" ::"v"(voff), "s"(sbase), "s"(lds) : "memory", "m0");
}
__device__ __forceinline__ void touch_lp(const void* p, unsigned lds) {
  asm volatile("s_mov_b32 m0, %1\n\ts_nop 0\n\tglobal_load_lds_dword %0, off" ::"v"(p), "s"(lds) : "memory", "m0");
}
#define POLA " sc1"
#define POLH " sc1"
__device__ __forceinline__ void st16a(void* p, u32x4 v) {
  asm volatile("global_store_dwordx4 %0, %1, off" POLA "\n\ts_nop 1" ::"v"(p), "v"(v) : "memory");
}
__device__ __forceinline__ void st16h(void* p, u32x4 v) {
  asm volatile("global_store_dwordx4 %0, %1, off" POLH "\n\ts_nop 1" ::"v"(p), "v"(v) : "memory");
}
#define PIN(v) asm volatile("" : "+v"(v))
#define STAT_ST 4

__device__ __forceinline__ float smul(float a, float b) {
  float d;
  asm("v_mul_f32 %0, %1, %2" : "=v"(d) : "v"(a), "v"(b));
  return d;
}
__device__ __forceinline__ float sadd(float a, float b) {
  float d;
  asm("v_add_f32 %0, %1, %2" : "=v"(d) : "v"(a), "v"(b));
  return d;
}
__device__ __forceinline__ float sfma(float a, float b, float c) {
  float d;
  asm("v_fma_f32 %0, %1, %2, %3" : "=v"(d) : "v"(a), "v"(b), "v"(c));
  return d;
}
template <class F>
__device__ __forceinline__ void kv_unit(int id, int lane, const u16* __restrict__ kin, const u16* __restrict__ vin,
                                        const u16* __restrict__ shk, char* lb, char* __restrict__ ws, F&& hook) {
  const int hs = id / 10, cgp = id - hs * 10;
  const int alpha = hs >> 1, half = hs & 1;
  const int c8 = lane & 7, seg = lane >> 3;
  const int s0 = hs * 64 + seg * 8, d0 = cgp * 64 + c8 * 8;
  u32x4 kr[11], vr[8], sw4[4];
#pragma unroll
  for (int i = 0; i < 11; ++i) {
    int s = s0 - 3 + i;
    u32x4 t = *(const u32x4*)(kin + (size_t)max(s, 0) * D_ + d0);
    u32x4 z = {0u, 0u, 0u, 0u};
    kr[i] = (i >= 3 || s >= 0) ? t : z;
  }
#pragma unroll
  for (int i = 0; i < 8; ++i) vr[i] = *(const u32x4*)(vin + (size_t)(s0 + i) * D_ + d0);
#pragma unroll
  for (int i = 0; i < 4; ++i) sw4[i] = *(const u32x4*)(shk + (size_t)d0 * 4 + i * 8);
  hook();
  char* kb = lb + c8 * 1024 + (seg & 3) * 4;
#pragma unroll
  for (int e = 0; e < 8; ++e) {
    unsigned t01 = sw4[e >> 1][(e & 1) * 2], t23 = sw4[e >> 1][(e & 1) * 2 + 1];
    float w0 = bf_lo(t01), w1 = bf_hi(t01), w2 = bf_lo(t23), w3 = bf_hi(t23);
    float kf[11], r[8];
#pragma unroll
    for (int i = 0; i < 11; ++i) { unsigned t = kr[i][e >> 1]; kf[i] = (e & 1) ? bf_hi(t) : bf_lo(t); }
#pragma unroll
    for (int i = 0; i < 8; ++i) {
      unsigned t = vr[i][e >> 1];
      float vv = (e & 1) ? bf_hi(t) : bf_lo(t);
      float a = smul(kf[i], w3);
      a = sfma(kf[i + 1], w2, a);
      a = sfma(kf[i + 2], w1, a);
      a = bf16r(sfma(kf[i + 3], w0, a));
      r[i] = smul(a, vv);
    }
#pragma unroll
    for (int c = 0; c < 4; ++c)
      *(LAS unsigned*)(kb + e * 128 + (((seg >> 2) * 4 + c + c8) & 7) * 16) = pack_bf16(r[c], r[c + 4]);
  }
#pragma unroll
  for (int i = 0; i < 8; ++i) {
    int ch = i * 8 + seg;
    u32x4 v = *(LAS u32x4*)(lb + ch * 128 + ((c8 + i) & 7) * 16);
    st16a(ws + WS_KVP + ((size_t)(cgp * 64 + ch) * 16 + alpha) * 256 + kv_pos(half * 2 + (c8 >> 2), alpha, c8 & 3) * 16, v);
  }
}

__global__ __launch_bounds__(512, 2) void k1_kernel(const u16* __restrict__ x, const u16* __restrict__ kin,
                                                    const u16* __restrict__ vin, const u16* __restrict__ shk,
                                                    const u16* __restrict__ wg, const u16* __restrict__ wu,
                                                    const u16* __restrict__ wd, const u16* __restrict__ ssmk,
                                                    const u16* __restrict__ ssmd, char* __restrict__ ws) {
  extern __shared__ __attribute__((aligned(16))) char smem[];
  const int b = blockIdx.x;
  const int w = __builtin_amdgcn_readfirstlane(threadIdx.x >> 6);
  const int lane = threadIdx.x & 63;
  const unsigned sbase = (unsigned)(size_t)(LAS char*)smem;
  const unsigned flg = sbase + K1_FLG;
  const int xcd = b & 7, bi = b >> 3;
  STAMP_RT(K1_DBG, 0);
  STAMP(K1_DBG, 1);
#define STAMPW(idx)

  const unsigned scr = sbase + K1_SCR;
  if (w == 7 && bi < 5) {
    unsigned long long pc;
    int l0, l1, l2;
    asm volatile("s_getpc_b64 %0\n.Lcode_a:\n\ts_mov_b32 %1, .Lcode_e0-.Lcode_a\n\ts_mov_b32 %2, .Lcode_e1-.Lcode_a\n\t"
                 "s_mov_b32 %3, .Lcode_e2-.Lcode_a" : "=s"(pc), "=s"(l0), "=s"(l1), "=s"(l2));
    touch_l((unsigned)min((bi * 64 + lane) * 128, max(max(l0, l1), max(l2, 0))), (const void*)pc, scr);
  }

  if (__builtin_expect(bi >= 27, 0)) {
    const int sl = (bi - 27) * 8 + w;
#define SPF_DELAY 16
    __builtin_amdgcn_s_sleep(SPF_DELAY);
    STAMP(K1_DBG, 3);
#pragma unroll
    for (int r = 0; r < 2; ++r) {
      const int id = w * 40 + xcd * 5 + bi - 27 + 320 * r;
      if (id < 224) {
        const int line = id * 64 + lane, row = line / 7;
        touch_l((unsigned)(row * (D_ * 2) + 384 + (line - row * 7) * 128), x, scr);
      } else if (id < 602) {
        const int j = id - 224;
        touch_l((unsigned)(192 * F_ * 2 + ((j >> 1) * 64 + lane) * 128), (j & 1) ? wu : wg, scr);
      }
    }
    const bool pk = sl < 2 && lane < 40;
    const int pd = (xcd * 2 + (sl & 1)) * 40 + min(lane, 39);
    u32x4 pt = {0u, 0u, 0u, 0u};
    unsigned pdv = 0u;
    if (sl < 2) {
      pt = *(const u32x4*)(ssmk + (size_t)pd * S_);
      pdv = ssmd[pd];
    }
    __builtin_amdgcn_s_sleep(SD2 - 24);
    if (pk) {
      const __bf16 k0 = (__bf16)(bf_lo(pdv) + bf_lo(pt.x));
      pt.x = (pt.x & 0xffff0000u) | (unsigned)__builtin_bit_cast(u16, k0);
      st16a(ws + WS_K0 + (size_t)pd * 16, pt);
    }
    __builtin_amdgcn_s_sleep(24);
    STAMP(K1_DBG, 4);
    kv_unit(xcd * 40 + sl, lane, kin, vin, shk, smem + w * 16384, ws, []() {});
    STAMP(K1_DBG, 6);
    asm volatile("s_waitcnt vmcnt(0)" ::: "memory");
    STAMP(K1_DBG, 2);
    STAMP_RT(K1_DBG, 31);
    STAMP_OUT(K1_DBG, 0);
    return;
  }

  const int gi = (xcd & 3) * 27 + bi;
  const int fu = gi >> 2;
  const int mt = (xcd >> 2) * 4 + (gi & 3);
  const int koff = 0;
  const unsigned xo0 = (unsigned)((lane >> 3) * (D_ * 2) + (((lane & 7) ^ (lane >> 4)) * 16));
  const unsigned wo0 = (unsigned)((lane >> 3) * (F_ * 2) + (((lane & 7) ^ (((lane >> 3) & 3) * 2)) * 16));
  const char* xs = (const char*)(x + (size_t)(mt * 256) * D_) + koff * 128;
  const char* gs = (const char*)(wg + (size_t)(koff * 64) * F_ + fu * 64);
  const char* us = (const char*)(wu + (size_t)(koff * 64) * F_ + fu * 64);
  if (w != 4 && w != 7) {
    const bool isx = w < 4;
    const char* src = isx ? xs + w * (64 * D_ * 2) : (w == 6 ? gs : us);
    const unsigned o0 = isx ? xo0 : (w == 6 ? wo0 : wo0 ^ 32u);
    const unsigned o1 = isx ? xo0 ^ 64u : o0;
    const unsigned st = (isx ? 8 * D_ * 2 : 8 * F_ * 2) - 1024;
    const unsigned dst = sbase + (isx ? STGW + w * 8192 : (6 - w) * 8192);
    set_m0(dst);
    glds_i<0>(o0, src);
    glds_i<1024>(o1 + st, src);
    glds_i<2048>(o0 + 2 * st, src);
    glds_i<3072>(o1 + 3 * st, src);
    set_m0(dst + 4096);
    glds_i<0>(o0 + 4 * st + 4096, src);
    glds_i<1024>(o1 + 5 * st + 4096, src);
    glds_i<2048>(o0 + 6 * st + 4096, src);
    glds_i<3072>(o1 + 7 * st + 4096, src);
  }
  const unsigned tst = ((lane & 31) < 16) ? 128u : (unsigned)(64 * F_ * 2);
  const char* tp;
  {
    const int xi = min(fu * 10 + (lane & 15), 255), wi = mt * 16 + (lane & 15);
    tp = ((lane & 31) < 16) ? xs + xi * (D_ * 2) : ((wi & 1) ? us : gs) + (size_t)(wi >> 1) * (F_ * 2);
  }
  if (w == 4) {
    touch_lp(tp + (1 + (lane >> 5)) * tst, scr);
    *(LAS unsigned*)(flg + lane * 4) = 0u;
  }
  __builtin_amdgcn_s_waitcnt(0xC07F);
  SBAR();
  BARRIER();
  SBAR();

  if (w == 7) {
    const int gb = (xcd >> 2) * 108 + gi;
#define PR7 1
#define PRL 3
#define PRM 2
    __builtin_amdgcn_s_setprio(PR7);
    while ((unsigned)__builtin_amdgcn_readfirstlane(lds_flag(flg + 4 * MK_ST)) < 1u) __builtin_amdgcn_s_sleep(8);
    {
      const u32x4 mk = {~0u, ~0u, ~0u, ~0u};
#pragma unroll
      for (int i = 0; i < 12; ++i)
        if (gb * 12 + i < 2576) st16a(ws + WS_YT + (size_t)(gb * 12 + i) * 1024 + lane * 16, mk);
    }
    while ((unsigned)__builtin_amdgcn_readfirstlane(lds_flag(flg + 4 * KV_ST)) < 1u) __builtin_amdgcn_s_sleep(4);
    asm volatile("" ::: "memory");
    STAMP(K1_DBG, 4);
    {
      const int ks = gb >> 2, nq = gb & 3;
#pragma unroll
      for (int i = 0; i < 10; ++i) {
        const int id = i * 64 + lane, r = id / 20;
        glds16_asm(wd + (size_t)(ks * 32 + r) * D_ + nq * 160 + (id - r * 20) * 8, sbase + K1_KV + i * 1024);
      }
      touch_l((unsigned)min(gb * 95 + lane, 20479) * 128u, ssmk, scr);
      touch_l((unsigned)min(gb * 95 + 64 + (lane & 31), 20479) * 128u, ssmk, scr);
      asm volatile("s_waitcnt vmcnt(2)" ::: "memory");
      STAMP(K1_DBG, 5);
      const int g = lane >> 4, q = (lane & 15) >> 2, p = lane & 3;
      const unsigned o = sbase + (unsigned)(K1_KV + (16 * (g >> 1) + 4 * (g & 1) + q) * 320 + p * 8);
#pragma unroll
      for (int n = 0; n < 10; ++n) {
        u32x2 lo = tr_read_a(o + n * 32);
        u32x2 hi = tr_read_a(o + n * 32 + 8 * 320);
        u32x4 v = {lo.x, lo.y, hi.x, hi.y};
        st16a(ws + WS_WDT + ((size_t)((nq * 10 + n) * NKS2 + ks) * 64 + lane) * 16, v);
      }
    }
    STAMP(K1_DBG, 6);
    asm volatile("s_waitcnt vmcnt(0)" ::: "memory");
    STAMP(K1_DBG, 7);
    STAMP(K1_DBG, 2);
    STAMP_RT(K1_DBG, 31);
    STAMP_OUT(K1_DBG, 0);
    return;
  }
  if (w >= 4) {
    const int lw = w - 4;
    __builtin_amdgcn_s_setprio(PRL);
    tp += min(2 * (lw ? 1 + lw : 4) + TOPT + (lane >> 5), 9) * tst;
    unsigned xv[32], wv[16];
#pragma unroll
    for (int i = 0; i < 32; ++i) xv[i] = ((i & 1) ? (xo0 ^ 64u) : xo0) + (unsigned)(i * (8 * D_ * 2) - (i & 3) * 1024);
#pragma unroll
    for (int i = 0; i < 16; ++i) wv[i] = ((i & 8) ? (wo0 ^ 32u) : wo0) + (unsigned)((i & 7) * (8 * F_ * 2) - (i & 3) * 1024);
    int kc = koff + 1 + lw;
    int adv = 1 + lw;
    if (kc >= 10) { kc -= 10; adv -= 10; }
    const unsigned lws = sbase + ((lw == 2) ? 0 : (1 + lw) * STGB);
#pragma unroll 1
    for (int s = 1 + lw; s < 10; s += 3) {
      xs += adv * 128;
      gs += adv * (64 * F_ * 2);
      us += adv * (64 * F_ * 2);
      if (s == 2 || s == 3) {
        if (ET) asm volatile("s_waitcnt vmcnt(0)" ::: "memory"); else asm volatile("s_waitcnt vmcnt(1)" ::: "memory");
        lds_signal(flg);
      }
      if (s >= 2) lds_wait_sleep(flg + 64 + (s - 3) * 4, (s == 2) ? 1u : 4u);
#pragma unroll
      for (int j = 0; j < 4; ++j) {
        set_m0(lws + j * 4096);
        const char* src = (j < 2) ? gs : us;
        glds_i<0>(wv[4 * j], src);
        glds_i<1024>(wv[4 * j + 1], src);
        glds_i<2048>(wv[4 * j + 2], src);
        glds_i<3072>(wv[4 * j + 3], src);
      }
#pragma unroll
      for (int j = 0; j < 8; ++j) {
        set_m0(lws + STGW + j * 4096);
        glds_i<0>(xv[4 * j], xs);
        glds_i<1024>(xv[4 * j + 1], xs);
        glds_i<2048>(xv[4 * j + 2], xs);
        glds_i<3072>(xv[4 * j + 3], xs);
      }
      if (s == 1 || s == 9) lds_signal(flg + (s == 1 ? 60 : 56));
      STAMP(K1_DBG, 13 + s);
      if (TOPT < 2 && s >= 2 && s <= 4) {
        touch_lp(tp, scr);
        asm volatile("s_waitcnt vmcnt(1)" ::: "memory");
      } else {
        asm volatile("s_waitcnt vmcnt(0)" ::: "memory");
      }
      lds_signal(flg + s * 4);
      STAMP(K1_DBG, 4 + s);
      adv = (kc >= 7) ? -7 : 3;
      kc += adv;
    }
    asm volatile("s_waitcnt vmcnt(0)" ::: "memory");
    STAMPW(26);
    STAMP(K1_DBG, 2);
    STAMP_RT(K1_DBG, 31);
    STAMP_OUT(K1_DBG, 0);
    return;
  }

  __builtin_amdgcn_s_setprio(PRM);
  const int hi = lane >> 5, q = (lane & 15) >> 2, p = lane & 3, mat = (lane >> 4) & 1;
  const int r2 = w >> 1, c2 = w & 1;
  unsigned roff[2];
#pragma unroll
  for (int j = 0; j < 2; ++j)
    roff[j] = sbase + (unsigned)(mat * 8192 + (8 * hi + q) * 128 + (((2 * c2 + (j ^ r2)) ^ q ^ mat) * 32) + p * 8);
  unsigned aoff[2][4];
#pragma unroll
  for (int kq = 0; kq < 4; ++kq) {
    unsigned o = sbase + (unsigned)(STGW + r2 * 16384 + (lane & 31) * 128 + (((2 * kq + hi) ^ ((lane >> 1) & 7)) * 16));
    aoff[0][kq] = o + c2 * 8192;
    aoff[1][kq] = o + (1 - c2) * 8192;
  }

  f32x16 acc[2][4];
#pragma unroll
  for (int c = 0; c < 8; ++c)
#pragma unroll
    for (int e = 0; e < 16; ++e) acc[c >> 2][c & 3][e] = 0.f;
  f32x4 accc = {0.f, 0.f, 0.f, 0.f}, accs[2] = {{0.f, 0.f, 0.f, 0.f}, {0.f, 0.f, 0.f, 0.f}};
  const unsigned sv = ((lane & 15) == mat) ? 0x3f803f80u : 0u;
  u32x4 sel = {sv, sv, sv, sv};
  asm volatile("" : "+v"(sel), "+v"(accc), "+v"(accs[0]), "+v"(accs[1]), "+v"(roff[0]), "+v"(roff[1]));
#pragma unroll
  for (int c = 0; c < 8; ++c) asm volatile("" : "+v"(acc[c >> 2][c & 3]));
#pragma unroll
  for (int c = 0; c < 4; ++c) asm volatile("" : "+v"(aoff[0][c]), "+v"(aoff[1][c]));
  unsigned fa = flg;
  STAMP(K1_DBG, 2);
  asm volatile("s_waitcnt vmcnt(0)" ::: "memory");
  lds_signal(fa);
  lds_wait(fa, 0u, 6u WARG);
  STAMP(K1_DBG, 3);
  STAMPW(25);

#define RA_ 12
#ifndef FLT_
#define FLT_ 24
#endif
  constexpr int RA = RA_;
#define HASF(j) ((j) != 5 && (j) != 7)
  auto ldf = [&](int tn) __attribute__((always_inline)) -> u32x4 {
    const int kn = (tn >> 3) & 3, jn = tn & 7;
    if ((jn & 1) == 0) return lds_rd128(aoff[jn >> 2][kn] + ((jn >> 1) & 1) * 4096);
    u32x2 lo = tr_read_a(roff[jn >> 1] + kn * 2048);
    u32x2 hh = tr_read_a(roff[jn >> 1] + kn * 2048 + 512);
    return (u32x4){lo.x, lo.y, hh.x, hh.y};
  };
  u32x4 cw[RA];
#pragma unroll
  for (int c = 0; c < RA; ++c) {
    if (HASF(c & 7)) cw[c] = ldf(c);
    SBAR();
  }
  int slot = 0;
#pragma unroll 1
  for (int s = 0; s < 10; ++s) {
    const unsigned dl = (slot == 2) ? (unsigned)(-2 * STGB) : (unsigned)STGB;
    slot = (slot == 2) ? 0 : slot + 1;
    const unsigned need = (s < 9) ? 1u : 0u;
    u32x4 fr[32 + RA];
    unsigned fl = 0u;
#pragma unroll
    for (int c = 0; c < RA; ++c)
      if (HASF(c & 7)) fr[c] = cw[c];
#pragma unroll
    for (int t = 0; t < 32; ++t) {
      const int kq = t >> 3, m = t & 7;
      const int tn = t + RA, kn = (tn >> 3) & 3, jn = tn & 7;
      if (tn == 32) lds_wait(fa + 4, fl, need WARG);
      if (HASF(jn)) {
        fr[tn] = ldf(tn);
        if (tn < 32) {
          if (jn == 2 || jn == 6) { aoff[jn >> 2][kn] += dl; PIN(aoff[jn >> 2][kn]); }
          else if ((jn & 1) && kn == 3) { roff[jn >> 1] += dl; PIN(roff[jn >> 1]); }
        }
      }
      if (t < RA && (m == 2 || m == 6)) { aoff[m >> 2][kq] += dl; PIN(aoff[m >> 2][kq]); }
      if (tn == 30) lds_signal(fa + 64);
      if (tn == FLT_) fl = lds_flag(fa + 4);
      SBAR();
      {
        const int tp = (m < 4) ? (m >> 1) : (m & 1);
        const int sp = (m < 4) ? (m & 1) : (m >> 1);
        acc[tp][sp] = mfma32(fr[8 * kq + 1 + 2 * tp], fr[8 * kq + 2 * sp], acc[tp][sp]);
        if (m == 3) {
          accc = mfma16(fr[8 * kq + 1], sel, accc);
          accs[0] = mfma16(sel, fr[8 * kq], accs[0]);
          accs[1] = mfma16(sel, fr[8 * kq + 2], accs[1]);
        }
      }
      SBAR();
    }
#pragma unroll
    for (int c = 0; c < RA; ++c)
      if (HASF(c & 7)) cw[c] = fr[32 + c];
    fa += 4;
  }
  STAMPW(26);

  STAMP(K1_DBG, 23);
  float* csb = (float*)(smem + K1_CS);
  if ((lane & 15) < 2) *(f32x4*)(csb + (2 * c2 + r2) * 32 + (lane & 15) * 16 + 4 * (lane >> 4)) = accc;
  float nm[4];
#pragma unroll
  for (int i = 0; i < 2; ++i) {
    float v0 = __shfl(accs[i][0], lane & 15), v1 = __shfl(accs[i][1], lane & 15);
    nm[i] = -bf16r(((lane & 16) ? v1 : v0) * (1.0f / D_));
  }
  csb[128 + r2 * 128 + c2 * 64 + lane] = hi ? nm[1] : nm[0];
  lds_signal(flg + 128);
  lds_wait(flg + 128, 0u, 4u WARG);
  nm[2] = csb[128 + r2 * 128 + (1 - c2) * 64 + (lane & 31)];
  nm[3] = csb[128 + r2 * 128 + (1 - c2) * 64 + 32 + (lane & 31)];
  char* hb = ws + WS_HT + ((size_t)((mt * 16 + r2 * 8 + mat) * NKS2 + 2 * fu + c2) * 64 + (lane & 15) + 16 * hi) * 16;
#pragma unroll
  for (int j = 0; j < 2; ++j) {
    const int tb = j ^ r2;
    const float* cp = csb + (2 * c2 + tb) * 32 + 4 * hi;
    f32x4 c4[4] = {*(const f32x4*)(cp), *(const f32x4*)(cp + 8), *(const f32x4*)(cp + 16), *(const f32x4*)(cp + 24)};
#pragma unroll
    for (int u = 0; u < 4; ++u) {
      const f32x2 nm2 = {nm[u], nm[u]};
      u32x4 o;
#pragma unroll
      for (int i = 0; i < 4; ++i) {
        f32x2 gv = {acc[j][u][2 * i], acc[j][u][2 * i + 1]}, uv = {acc[j][u][8 + 2 * i], acc[j][u][9 + 2 * i]};
        f32x2 cgv = {c4[i >> 1][(2 * i) & 3], c4[i >> 1][(2 * i + 1) & 3]};
        f32x2 cuv = {c4[2 + (i >> 1)][(2 * i) & 3], c4[2 + (i >> 1)][(2 * i + 1) & 3]};
        gv = __builtin_elementwise_fma(nm2, cgv, gv);
        uv = __builtin_elementwise_fma(nm2, cuv, uv);
        f32x2 hv = gv * uv;
        o[i] = pack_bf16(hv.x, hv.y);
      }
      st16h(hb + (size_t)((u ^ (2 * c2)) * 2 * NKS2) * 1024 + tb * 512, o);
    }
  }
  STAMP(K1_DBG, 24);
  STAMP_RT(K1_DBG, 31);
  STAMP_OUT(K1_DBG, 0);
}

constexpr int K2_LDS = 4 * 20 * 1024;
constexpr int NCONV = 160;

template <int OFF>
__device__ __forceinline__ void gload16s(u32x4& dst, unsigned voff, const void* sbase) {
  asm volatile("global_load_dwordx4 %0, %1, %2 offset:%3" : "=v"(dst) : "v"(voff), "s"(sbase), "i"(OFF) : "memory");
}
template <int OFF>
__device__ __forceinline__ void gload8s(u32x2& dst, unsigned voff, const void* sbase) {
  asm volatile("global_load_dwordx2 %0, %1, %2 offset:%3" : "=v"(dst) : "v"(voff), "s"(sbase), "i"(OFF) : "memory");
}
template <int OFF>
__device__ __forceinline__ void gload8c(unsigned long long& dst, unsigned voff, const void* sbase) {
  asm volatile("global_load_dwordx2 %0, %1, %2 offset:%3 sc1" : "=v"(dst) : "v"(voff), "s"(sbase), "i"(OFF) : "memory");
}
template <int N>
__device__ __forceinline__ float dpp_shr(float v) {
  return __builtin_bit_cast(float, __builtin_amdgcn_update_dpp(0, __builtin_bit_cast(int, v), 0x110 + N, 0xf, 0xf, true));
}
template <int N>
__device__ __forceinline__ float dpp_shl(float v) {
  return __builtin_bit_cast(float, __builtin_amdgcn_update_dpp(0, __builtin_bit_cast(int, v), 0x100 + N, 0xf, 0xf, true));
}

__global__ __launch_bounds__(256, 2) void k2_kernel(const u16* __restrict__ x, const u16* __restrict__ ssmk, char* ws,
                                                    u16* __restrict__ out) {
  extern __shared__ __attribute__((aligned(16))) char smem[];
  const int w = __builtin_amdgcn_readfirstlane(threadIdx.x >> 6);
  const int lane = threadIdx.x & 63;
  const int g = lane >> 4;
  ST2_DECL;
  ST2_RT(0);
  ST2(1);
  unsigned ct = 0u;

  if (blockIdx.x < NCONV) {
    __builtin_amdgcn_s_setprio(3);
    const int cb = blockIdx.x;
    const int d = cb * 4 + w;
    char* lb = smem + w * 9216;
    const unsigned lbase = (unsigned)(size_t)(LAS char*)lb;
#pragma unroll
    for (int i = 0; i < 4; ++i) {
      glds16_asm((i == 3 && lane == 0) ? (const void*)(ws + WS_K0 + (size_t)d * 16)
                                       : (const void*)(ssmk + (size_t)d * S_ + (3 - i) * 512 + lane * 8),
                 lbase + 256 + (3 - i) * 1024);
      glds16_asm(ws + WS_KVP + (size_t)d * 4096 + i * 1024 + lane * 16, lbase + 5120 + i * 1024);
    }
    {
      u32x4 z = {0u, 0u, 0u, 0u};
      *(LAS u32x4*)(lb + ((lane < 16) ? lane * 16 : 4352 - 256 + lane * 16)) = z;
    }

    const int q = (lane & 15) >> 2, p = lane & 3;
    f32x16 acc[8];
#pragma unroll
    for (int t = 0; t < 8; ++t)
#pragma unroll
      for (int e = 0; e < 16; ++e) acc[t][e] = 0.f;
    unsigned ka = lbase + (unsigned)(256 + 32 + 32 * (g & 1) - 64 * (g >> 1) - 8 * q + 8 * p);
    unsigned xa = lbase + (unsigned)(5120 - 448 + 16 * (lane & 31) + 64 * (lane >> 5));
    asm volatile("" : "+v"(ka), "+v"(xa));
#pragma unroll
    for (int t = 0; t < 8; ++t) asm volatile("" : "+v"(acc[t]));
    asm volatile("s_waitcnt vmcnt(6) lgkmcnt(0)" : "+v"(ct)::"memory");
    asm volatile("" ::: "memory");
    ST2(2);
    u32x4 xf[4][8], kf[4][8];
#define RS(p) (((p) + 1) & 3)
#define WV(n) do { SBAR(); asm volatile("s_waitcnt vmcnt(" #n ")" ::: "memory"); } while (0)
#define LX(r, d) xf[r][d] = lds_rd128(xa + 512 * (d) + 128 * (3 - RS(r)))
#define LK(r, i)                                                                                   \
  do {                                                                                             \
    u32x2 lo_ = tr_read_a(ka + 128 * RS(r) + 512 * (i) + 32), hi_ = tr_read_a(ka + 128 * RS(r) + 512 * (i)); \
    kf[r][i] = (u32x4){lo_.x, lo_.y, hi_.x, hi_.y};                                                \
  } while (0)
#define MM(r, i, d)                                                                                \
  do {                                                                                             \
    SBAR();                                                                                        \
    acc[(i) + (d)] = mfma32(xf[r][d], kf[r][i], acc[(i) + (d)]);                                   \
    SBAR();                                                                                        \
  } while (0)
    LK(0, 7); LX(0, 0); LK(0, 6); LX(0, 1);
    MM(0, 7, 0); WV(5); LK(0, 5); LK(0, 4); MM(0, 6, 0); MM(0, 6, 1);
    MM(0, 5, 0); WV(4); LX(0, 2); LX(0, 3); MM(0, 5, 1); MM(0, 4, 0); MM(0, 4, 1);
    MM(0, 5, 2); WV(3); LK(0, 3); LK(0, 2); MM(0, 4, 2); MM(0, 4, 3);
    MM(0, 3, 0); MM(0, 3, 1); MM(0, 3, 2); MM(0, 3, 3); MM(0, 2, 0); WV(2); LX(0, 4); LX(0, 5); MM(0, 2, 1); MM(0, 2, 2);
    MM(0, 2, 3); WV(1); LK(0, 1); LK(0, 0); MM(0, 3, 4); MM(0, 2, 4); MM(0, 2, 5);
    MM(0, 1, 0); MM(0, 1, 1); MM(0, 1, 2); WV(0); LX(0, 6); MM(0, 1, 3); MM(0, 1, 4);
    MM(0, 1, 5); LX(0, 7); MM(0, 1, 6); LK(1, 7); LX(1, 0); MM(0, 0, 0); LK(1, 6); MM(0, 0, 1);
    LX(1, 1); MM(0, 0, 2); LK(1, 5); MM(0, 0, 3); MM(0, 0, 4); LX(1, 2); MM(0, 0, 5); LK(1, 4); MM(0, 0, 6);
    MM(0, 0, 7); if (CSW == 2) __builtin_amdgcn_s_setprio(CPRIO); MM(1, 7, 0); LX(1, 3); MM(1, 6, 0); LK(1, 3); MM(1, 6, 1); MM(1, 5, 0); MM(1, 5, 1); MM(1, 5, 2);
    LX(1, 4); MM(1, 4, 0); LK(1, 2); MM(1, 4, 1); MM(1, 4, 2); MM(1, 4, 3); MM(1, 3, 0); MM(1, 3, 1);
    LX(1, 5); MM(1, 3, 2); LK(1, 1); MM(1, 3, 3); MM(1, 3, 4); if (CSW == 3) __builtin_amdgcn_s_setprio(CPRIO); MM(1, 2, 0); MM(1, 2, 1); MM(1, 2, 2); MM(1, 2, 3);
    LX(1, 6); MM(1, 2, 4); LK(1, 0); MM(1, 2, 5); MM(1, 1, 0); MM(1, 1, 1); MM(1, 1, 2); MM(1, 1, 3); MM(1, 1, 4);
    MM(1, 1, 5); LX(1, 7); MM(1, 1, 6); LK(2, 7); LX(2, 0); MM(1, 0, 0); LK(2, 6); MM(1, 0, 1);
    LX(2, 1); MM(1, 0, 2); LK(2, 5); MM(1, 0, 3); MM(1, 0, 4); LX(2, 2); MM(1, 0, 5); LK(2, 4); MM(1, 0, 6);
    MM(1, 0, 7); if (CSW == 4) __builtin_amdgcn_s_setprio(CPRIO); MM(2, 7, 0); LX(2, 3); MM(2, 6, 0); LK(2, 3); MM(2, 6, 1); MM(2, 5, 0); MM(2, 5, 1); MM(2, 5, 2);
    LX(2, 4); MM(2, 4, 0); LK(2, 2); MM(2, 4, 1); MM(2, 4, 2); MM(2, 4, 3); MM(2, 3, 0); MM(2, 3, 1);
    LX(2, 5); MM(2, 3, 2); LK(2, 1); MM(2, 3, 3); MM(2, 3, 4); if (CSW == 5) __builtin_amdgcn_s_setprio(CPRIO); MM(2, 2, 0); MM(2, 2, 1); MM(2, 2, 2); MM(2, 2, 3);
    LX(2, 6); MM(2, 2, 4); LK(2, 0); MM(2, 2, 5); MM(2, 1, 0); MM(2, 1, 1); MM(2, 1, 2); MM(2, 1, 3); MM(2, 1, 4);
    MM(2, 1, 5); LX(2, 7); MM(2, 1, 6); LK(3, 7); LX(3, 0); MM(2, 0, 0); LK(3, 6); MM(2, 0, 1);
    LX(3, 1); MM(2, 0, 2); LK(3, 5); MM(2, 0, 3); MM(2, 0, 4); LX(3, 2); MM(2, 0, 5); LK(3, 4); MM(2, 0, 6);
    MM(2, 0, 7); if (CSW == 6) __builtin_amdgcn_s_setprio(CPRIO); MM(3, 7, 0); LX(3, 3); MM(3, 6, 0); LK(3, 3); MM(3, 6, 1); MM(3, 5, 0); MM(3, 5, 1); MM(3, 5, 2);
    LX(3, 4); MM(3, 4, 0); LK(3, 2); MM(3, 4, 1); MM(3, 4, 2); MM(3, 4, 3); MM(3, 3, 0); MM(3, 3, 1);
    LX(3, 5); MM(3, 3, 2); LK(3, 1); MM(3, 3, 3); MM(3, 3, 4); if (CSW == 7) __builtin_amdgcn_s_setprio(CPRIO); MM(3, 2, 0); MM(3, 2, 1); MM(3, 2, 2); MM(3, 2, 3);
    LX(3, 6); MM(3, 2, 4); LK(3, 0); MM(3, 2, 5); MM(3, 1, 0); MM(3, 1, 1); MM(3, 1, 2); MM(3, 1, 3); MM(3, 1, 4);
    MM(3, 1, 5); LX(3, 7); MM(3, 1, 6); MM(3, 0, 0); MM(3, 0, 1); MM(3, 0, 2); MM(3, 0, 3); MM(3, 0, 4);
    MM(3, 0, 5); MM(3, 0, 6); MM(3, 0, 7);
    ST2(3);
    __builtin_amdgcn_s_setprio(CPRIO2);
    asm volatile("s_nop 7\n\ts_nop 7\n\ts_nop 7" ::: "memory");
    char* ab = smem + 40960 + lane * 8 + w * 2;
    {
      auto addup = [&](float z, float t) __attribute__((always_inline)) -> float {
        asm volatile("v_add_f32_dpp %0, %1, %0 wave_ror:1 row_mask:0xf bank_mask:0xf" : "+v"(z) : "v"(t));
        return z;
      };
      float pc[4] = {0.f, 0.f, 0.f, 0.f};
#pragma unroll
      for (int nb = 0; nb < 4; ++nb) {
        float s[8], t[8];
#pragma unroll
        for (int n = 0; n < 8; ++n) s[n] = acc[2 * nb + (n >> 2)][4 * (n & 3) + 3];
#pragma unroll
        for (int c = 3; c >= 1; --c) {
#pragma unroll
          for (int n = 0; n < 8; ++n) t[n] = (lane == 63) ? (n ? s[n ? n - 1 : 0] : pc[c]) : s[n];
          pc[c] = s[7];
          SBAR();
#pragma unroll
          for (int n = 0; n < 8; ++n) s[n] = addup(acc[2 * nb + (n >> 2)][4 * (n & 3) + c - 1], t[n]);
          SBAR();
        }
#pragma unroll
        for (int n = 0; n < 8; n += 2) {
          unsigned pk = pack_bf16(s[n], s[n + 1]);
          *(LAS u16*)(ab + (nb * 8 + n) * 512) = (u16)pk;
          *(LAS u16*)(ab + (nb * 8 + n) * 512 + 512) = (u16)(pk >> 16);
        }
      }
    }
    ST2(4);
    __builtin_amdgcn_s_waitcnt(0xC07F);
    BARRIER();
    {
      const unsigned yo = (unsigned)(WS_YT + ((threadIdx.x >> 3) * 40 + (cb >> 2)) * 512 + (cb & 3) * 128 + (threadIdx.x & 7) * 16);
#pragma unroll
      for (int i = 0; i < 4; ++i) {
        u32x4 yv = *(LAS u32x4*)(smem + 40960 + i * 4096 + threadIdx.x * 16);
        if ((yv.x & yv.y) == ~0u) yv.x = ~1u;
        if ((yv.z & yv.w) == ~0u) yv.z = ~1u;
        asm volatile("global_store_dwordx4 %0, %1, %2 sc1\n\ts_nop 1" ::"v"(yo + i * (32 * 40 * 512)), "v"(yv), "s"(ws) : "memory");
      }
    }
    asm volatile(".Lcode_e1:" ::: "memory");
    ST2(5);
    ST2(6);
    ST2_RT(11);
    ST2_OUT();
    return;
  }

  if (blockIdx.x >= NCONV + 256) {
    __builtin_amdgcn_s_setprio(3);
    const int srow = ((blockIdx.x - (NCONV + 256)) * 4 + w) * 8 + (lane >> 3);
    u32x4 xr[10];
#pragma unroll
    for (int i = 0; i < 10; ++i) xr[i] = *(const u32x4*)(x + (size_t)srow * D_ + (lane & 7) * 8 + i * 64);
    float s4[4] = {0.f, 0.f, 0.f, 0.f};
#pragma unroll
    for (int i = 0; i < 10; ++i)
#pragma unroll
      for (int e = 0; e < 4; ++e) s4[e] = sadd(s4[e], sadd(bf_lo(xr[i][e]), bf_hi(xr[i][e])));
    float sm = (s4[0] + s4[1]) + (s4[2] + s4[3]);
    sm += __shfl_xor(sm, 1);
    sm += __shfl_xor(sm, 2);
    sm += __shfl_xor(sm, 4);
    const float mean = sm * (1.0f / D_);
    float q4[4] = {0.f, 0.f, 0.f, 0.f};
#pragma unroll
    for (int i = 0; i < 10; ++i)
#pragma unroll
      for (int e = 0; e < 4; ++e) {
        float e0 = sadd(bf_lo(xr[i][e]), -mean), e1 = sadd(bf_hi(xr[i][e]), -mean);
        q4[e] = sfma(e0, e0, q4[e]);
        q4[e] = sfma(e1, e1, q4[e]);
      }
    float sq = (q4[0] + q4[1]) + (q4[2] + q4[3]);
    sq += __shfl_xor(sq, 1);
    sq += __shfl_xor(sq, 2);
    sq += __shfl_xor(sq, 4);
    const float var = sq * (1.0f / D_);
    if ((lane & 7) == 0) {
      f32x2 st = {bf16r(mean), bf16r(sqrtf(bf16r(bf16r(var) + 1e-5f)))};
      __hip_atomic_store((GAS unsigned long long*)(ws + WS_STATS + (size_t)srow * 8), __builtin_bit_cast(unsigned long long, st),
                         __ATOMIC_RELAXED, __HIP_MEMORY_SCOPE_AGENT);
    }
    asm volatile(".Lcode_e2:" ::: "memory");
    return;
  }

  const int b = blockIdx.x - NCONV;
  const int xcd = b & 7, bi = b >> 3;
  const int mt = (xcd >> 1) * 8 + (bi >> 2);
  const int nt = (xcd & 1) * 4 + (bi & 3);
  const int ksf = (w < 2) ? w * 14 : 27 + (w - 2) * 13;
  const char* hp = ws + WS_HT + (size_t)(mt * 4 * NKS2 + ksf) * 1024;
  const char* wp = ws + WS_WDT + (size_t)(nt * 5 * NKS2 + ksf) * 1024;
  const int m16 = mt * 4 + w;
  const int row = m16 * 16 + (lane & 15);

  u32x2 xv[5];
  {
    const unsigned xo = (unsigned)((row * D_ + nt * 80 + 4 * g) * 2);
    gload8s<0>(xv[0], xo, x);
    gload8s<32>(xv[1], xo, x);
    gload8s<64>(xv[2], xo, x);
    gload8s<96>(xv[3], xo, x);
    gload8s<128>(xv[4], xo, x);
  }

  f32x4 acc[4][5];
#pragma unroll
  for (int m = 0; m < 4; ++m)
#pragma unroll
    for (int n = 0; n < 5; ++n) acc[m][n] = (f32x4){0.f, 0.f, 0.f, 0.f};

  u32x4 hf[4][4], wf[4][5];
  unsigned vo[4];
#pragma unroll
  for (int i = 0; i < 4; ++i) vo[i] = (unsigned)(lane * 16 + i * 4096);
#define LOADK(j)                                                                           \
  do {                                                                                     \
    _Pragma("unroll") for (int m = 0; m < 4; ++m)                                          \
        gload16s<((j) & 3) * 1024>(hf[(j) & 3][m], vo[(j) >> 2], hp + (size_t)((w + m) & 3) * NKS2 * 1024); \
    _Pragma("unroll") for (int n = 0; n < 5; ++n)                                          \
        gload16s<((j) & 3) * 1024>(wf[(j) & 3][n], vo[(j) >> 2], wp + (size_t)n * NKS2 * 1024); \
  } while (0)
#define WAITK(j, N)                                                                        \
  asm volatile("s_waitcnt vmcnt(" #N ")"                                                   \
               : "+v"(hf[(j) & 3][0]), "+v"(hf[(j) & 3][1]), "+v"(hf[(j) & 3][2]), "+v"(hf[(j) & 3][3]), \
                 "+v"(wf[(j) & 3][0]), "+v"(wf[(j) & 3][1]), "+v"(wf[(j) & 3][2]), "+v"(wf[(j) & 3][3]), \
                 "+v"(wf[(j) & 3][4])::"memory")
#define MMAK(j)                                                                            \
  do {                                                                                     \
    _Pragma("unroll") for (int m = 0; m < 4; ++m)                                          \
        _Pragma("unroll") for (int n = 0; n < 5; ++n)                                      \
            acc[m][n] = mfma16(wf[(j) & 3][n], hf[(j) & 3][m], acc[m][n]);                 \
    SBAR();                                                                                \
  } while (0)
  const unsigned pfb = (unsigned)(size_t)(LAS char*)smem + (unsigned)(w * 20480);
#define DMAK(j)                                                                            \
  do {                                                                                     \
    _Pragma("unroll") for (int m = 0; m < 4; ++m)                                          \
        glds16_saddr(vo[0], hp + ((size_t)((w + m) & 3) * NKS2 + (j)) * 1024, pfb + (((j) & 1) * 9 + m) * 1024); \
    _Pragma("unroll") for (int n = 0; n < 5; ++n)                                          \
        glds16_saddr(vo[0], wp + ((size_t)n * NKS2 + (j)) * 1024, pfb + (((j) & 1) * 9 + 4 + n) * 1024); \
  } while (0)
#define LDSK(j)                                                                            \
  do {                                                                                     \
    _Pragma("unroll") for (int m = 0; m < 4; ++m)                                          \
        hf[(j) & 3][m] = lds_rd128(pfb + vo[0] + (((j) & 1) * 9 + m) * 1024);              \
    _Pragma("unroll") for (int n = 0; n < 5; ++n)                                          \
        wf[(j) & 3][n] = lds_rd128(pfb + vo[0] + (((j) & 1) * 9 + 4 + n) * 1024);          \
    SBAR();                                                                                \
  } while (0)
  if (w < 2) LOADK(0);
  LOADK(1);
  LOADK(2);
  LOADK(3);
  DMAK(12);
  DMAK(13);
  __builtin_amdgcn_s_setprio(GPRIO);
  if (w < 2) { WAITK(0, 45); MMAK(0); }
  LOADK(4);  WAITK(1, 45);  MMAK(1);
  LOADK(5);  WAITK(2, 45);  MMAK(2);
  LOADK(6);  WAITK(3, 45);  MMAK(3);
  LOADK(7);  WAITK(4, 27);  MMAK(4);
  LOADK(8);  WAITK(5, 27);  MMAK(5);
  LOADK(9);  WAITK(6, 27);  MMAK(6);
  const char* sp = ws + WS_STATS + (size_t)m16 * 128;
  unsigned long long sv;
  gload8c<0>(sv, (vo[0] >> 1) & 127u, sp);
  LOADK(10); WAITK(7, 28);  MMAK(7);
  LOADK(11); WAITK(8, 28);  MMAK(8);
  LDSK(12);  WAITK(9, 19);  MMAK(9);
  LDSK(13);  MMAK(12);
  WAITK(10, 9); MMAK(10);
  const char* yp = ws + WS_YT + (size_t)(m16 * 40 + nt * 5) * 512;
  const unsigned yo = vo[0] >> 1;
  asm volatile("" : "+v"(xv[0]), "+v"(xv[1]), "+v"(xv[2]), "+v"(xv[3]), "+v"(xv[4]), "+v"(sv));
  unsigned long long yv[5];
  gload8c<0>(yv[0], yo, yp);
  gload8c<512>(yv[1], yo, yp);
  gload8c<1024>(yv[2], yo, yp);
  gload8c<1536>(yv[3], yo, yp);
  gload8c<2048>(yv[4], yo, yp);
  SBAR();
  MMAK(13);
  f32x2 ob[5][2], xn[5][2], rs2;
#pragma unroll
  for (int n = 0; n < 5; ++n) {
    ob[n][0] = (f32x2){bf_lo(xv[n].x), bf_hi(xv[n].x)};
    ob[n][1] = (f32x2){bf_lo(xv[n].y), bf_hi(xv[n].y)};
    PIN(ob[n][0]);
    PIN(ob[n][1]);
  }
  auto normalise = [&]() __attribute__((always_inline)) {
    const float mean = __uint_as_float((unsigned)sv), rden = 1.0f / __uint_as_float((unsigned)(sv >> 32));
    const f32x2 nm2 = {-mean, -mean}, rd2 = {rden, rden};
    rs2 = (f32x2){rden * rden, rden * rden};
    PIN(rs2);
#pragma unroll
    for (int n = 0; n < 5; ++n) {
      xn[n][0] = (ob[n][0] + nm2) * rd2;
      xn[n][1] = (ob[n][1] + nm2) * rd2;
      PIN(xn[n][0]);
      PIN(xn[n][1]);
    }
  };
  normalise();
  unsigned ol[3], og[3];
#pragma unroll
  for (int i = 0; i < 3; ++i) {
    const int id = min(i * 64 + lane, 159), r = id / 10, c = id - r * 10;
    ol[i] = (unsigned)(r * 176 + c * 16);
    og[i] = (unsigned)((m16 * 16 + r) * (D_ * 2) + nt * 160 + c * 16);
    PIN(ol[i]);
    PIN(og[i]);
  }
  SBAR();
  WAITK(11, 5);
  SBAR();
#pragma unroll
  for (int jj = 1; jj < 6; ++jj) {
    if (jj < 5) {
#pragma unroll
      for (int n = 0; n < 5; ++n) acc[jj & 3][n] = mfma16(wf[3][n], hf[3][jj & 3], acc[jj & 3][n]);
    }
    SBAR();
    if (jj >= 2 && jj < 5) {
#pragma unroll
      for (int n = 0; n < 5; ++n) *(LAS f32x4*)(smem + ((w * 20 + (jj - 2) * 5 + n) * 64 + lane) * 16) = acc[jj - 1][n];
    }
    SBAR();
  }
  ST2(3);
  asm volatile("s_waitcnt vmcnt(0)" : "+v"(yv[0]), "+v"(yv[1]), "+v"(yv[2]), "+v"(yv[3]), "+v"(yv[4])::"memory");
  auto missing = [&]() __attribute__((always_inline)) -> bool {
    bool miss = sv == ~0ull;
#pragma unroll
    for (int n = 0; n < 5; ++n) miss |= (yv[n] == ~0ull);
    return __builtin_amdgcn_ballot_w64(miss) != 0;
  };
  if (__builtin_expect(missing(), 0)) {
    do {
      if (sv == ~0ull)
        sv = __hip_atomic_load((GAS unsigned long long*)(sp + (yo & 127u)), __ATOMIC_RELAXED, __HIP_MEMORY_SCOPE_AGENT);
#pragma unroll
      for (int n = 0; n < 5; ++n)
        if (yv[n] == ~0ull)
          yv[n] = __hip_atomic_load((GAS unsigned long long*)(yp + yo + n * 512), __ATOMIC_RELAXED, __HIP_MEMORY_SCOPE_AGENT);
      __builtin_amdgcn_s_sleep(PSL);
    } while (missing());
    normalise();
  }
#pragma unroll
  for (int n = 0; n < 5; ++n) {
    const unsigned y0 = (unsigned)yv[n], y1 = (unsigned)(yv[n] >> 32);
    ob[n][0] = __builtin_elementwise_fma(xn[n][0], (f32x2){bf_lo(y0), bf_hi(y0)}, ob[n][0]);
    ob[n][1] = __builtin_elementwise_fma(xn[n][1], (f32x2){bf_lo(y1), bf_hi(y1)}, ob[n][1]);
    ob[n][0] = __builtin_elementwise_fma((f32x2){acc[0][n][0], acc[0][n][1]}, rs2, ob[n][0]);
    ob[n][1] = __builtin_elementwise_fma((f32x2){acc[0][n][2], acc[0][n][3]}, rs2, ob[n][1]);
    PIN(ob[n][0]);
    PIN(ob[n][1]);
  }
  ST2(4);
  __builtin_amdgcn_s_waitcnt(0xC07F);
  __builtin_amdgcn_s_barrier();
  asm volatile("" ::: "memory");
  ST2(5);
  f32x4 pr[5][3];
#pragma unroll
  for (int n = 0; n < 5; ++n)
#pragma unroll
    for (int j = 0; j < 3; ++j) pr[n][j] = *(LAS f32x4*)(smem + ((((w + 3 - j) & 3) * 20 + j * 5 + n) * 64 + lane) * 16);
  SBAR();
#pragma unroll
  for (int n = 0; n < 5; ++n) {
    const f32x4 s = (pr[n][0] + pr[n][1]) + pr[n][2];
    f32x2 o0 = __builtin_elementwise_fma((f32x2){s[0], s[1]}, rs2, ob[n][0]);
    f32x2 o1 = __builtin_elementwise_fma((f32x2){s[2], s[3]}, rs2, ob[n][1]);
    u32x2 ov = {pack_bf16(o0.x, o0.y), pack_bf16(o1.x, o1.y)};
    *(LAS u32x2*)(smem + w * 20480 + 15360 + (lane & 15) * 176 + n * 32 + g * 8) = ov;
  }
#pragma unroll
  for (int i = 0; i < 3; ++i) {
    if (i < 2 || lane < 32) {
      u32x4 v = *(LAS u32x4*)(smem + w * 20480 + 15360 + ol[i]);
      asm volatile("global_store_dwordx4 %0, %1, %2" POLO "\n\ts_nop 1" ::"v"(og[i]), "v"(v), "s"(out) : "memory");
    }
  }
  asm volatile(".Lcode_e0:" ::: "memory");
  ST2(6);
  ST2_RT(11);
  ST2_OUT();
}

extern "C" void kernel_launch(void* const* d_in, const int* in_sizes, int n_in, void* d_out, int out_size, void* d_ws,
                              size_t ws_size, hipStream_t stream) {
  const u16* q = (const u16*)d_in[0];
  const u16* k = (const u16*)d_in[1];
  const u16* v = (const u16*)d_in[2];
  const u16* shk = (const u16*)d_in[3];
  const u16* ssmk = (const u16*)d_in[4];
  const u16* ssmd = (const u16*)d_in[5];
  const u16* wg = (const u16*)d_in[7];
  const u16* wu = (const u16*)d_in[8];
  const u16* wd = (const u16*)d_in[9];
  (void)hipFuncSetAttribute((const void*)k1_kernel, hipFuncAttributeMaxDynamicSharedMemorySize, K1_LDS);
  (void)hipFuncSetAttribute((const void*)k2_kernel, hipFuncAttributeMaxDynamicSharedMemorySize, K2_LDS);
  k1_kernel<<<256, 512, K1_LDS, stream>>>(q, k, v, shk, wg, wu, wd, ssmk, ssmd, (char*)d_ws);
  k2_kernel<<<NCONV + 256 + 64, 256, K2_LDS, stream>>>(q, ssmk, (char*)d_ws, (u16*)d_out);
}
```
